```python
import torch

def _softmax(x, axis=-1):
    return torch.softmax(x, dim=axis)
def _gelu(x):
    return torch.nn.functional.gelu(x, approximate="tanh")
def _silu(x):
    return torch.nn.functional.silu(x)
def _sigmoid(x):
    return torch.sigmoid(x)
def _layernorm(x, axis=-1, eps=1e-5):
    m = x.mean(dim=axis, keepdim=True)
    v = x.var(dim=axis, keepdim=True, unbiased=False)
    return (x - m) / torch.sqrt(v + eps)
def _scatter_add(tgt, idx, src, axis=0):
    return tgt.clone().index_add_(axis, idx, src)
def _segment_sum(src, seg, n):
    out = torch.zeros((n,) + src.shape[1:], dtype=src.dtype)
    return out.index_add_(0, seg, src)
def _pad(x, pw):
    flat = tuple(v for pair in reversed(pw) for v in pair)
    return torch.nn.functional.pad(x, flat)

kernel_name = 'sequential_preLN__mha_block__mixer_token_mlp__prompt'

def setup_inputs():

    _w = (lambda a: (lambda t: t.to(torch.bfloat16) if t.is_floating_point() else t)(torch.as_tensor(a)))
    def _A_setup():
        B, S, D, H = 1, 2048, 576, 32
        return {
            "x": _w((lambda *s: torch.randn(*s, dtype=torch.bfloat16) * 0.05)(B, S, D)),
            "wqkv": _w((lambda *s: torch.randn(*s, dtype=torch.bfloat16) * 0.05)(D, 3 * D)),
            "wo": _w((lambda *s: torch.randn(*s, dtype=torch.bfloat16) * 0.05)(D, D)),
        }
    _A_in = _A_setup()

    def _B_setup():
        B, S, D, F = 1, 2048, 576, 4608
        return {
            "x": _w((lambda *s: torch.randn(*s, dtype=torch.bfloat16) * 0.05)(B, S, D)),
            "w1": _w((lambda *s: torch.randn(*s, dtype=torch.bfloat16) * 0.8)(S, F) * (S ** -0.5)),
            "b1": _w((lambda *s: torch.randn(*s, dtype=torch.bfloat16) * 12.8)(F) * 0.02),
            "w2": _w((lambda *s: torch.randn(*s, dtype=torch.bfloat16) * 0.4)(F, S) * (F ** -0.5)),
            "b2": _w((lambda *s: torch.randn(*s, dtype=torch.bfloat16) * 6.4)(S) * 0.02),
        }
    _B_in = _B_setup()

    return {
        "A_x": _A_in["x"],
        "A_wqkv": _A_in["wqkv"],
        "A_wo": _A_in["wo"],
        "B_x": _B_in["x"],
        "B_w1": _B_in["w1"],
        "B_b1": _B_in["b1"],
        "B_w2": _B_in["w2"],
        "B_b2": _B_in["b2"],
    }

def reference(A_x, A_wqkv, A_wo, B_x, B_w1, B_b1, B_w2, B_b2):
    def _A_ref(x, wqkv, wo):
        B, S, D = x.shape
        H = 32
        Dh = D // H
        qkv = (lambda x, *d: x.permute(*d))((x @ wqkv).reshape(B, S, 3, H, Dh), 2, 0, 3, 1, 4)
        q, k, v = qkv[0], qkv[1], qkv[2]
        scores = torch.einsum("bhqd,bhkd->bhqk", q, k) * (Dh ** -0.5)
        p = _softmax(scores, axis=-1)
        o = torch.einsum("bhqk,bhkd->bhqd", p, v)
        return (lambda x, *d: x.permute(*d))(o, 0, 2, 1, 3).reshape(B, S, D) @ wo

    def _B_ref(x, w1, b1, w2, b2):
        B, S, D = x.shape
        h = _layernorm(x, axis=-1)
        u = torch.einsum("bsd,sf->bdf", h, w1) + b1
        u = _gelu(u)
        y = torch.einsum("bdf,fs->bds", u, w2) + b2
        return (lambda x, *d: x.permute(*d))(y, 0, 2, 1)

    x = A_x
    h = x + _A_ref(_layernorm(x, axis=-1), A_wqkv, A_wo)
    h2 = h + _B_ref(_layernorm(h, axis=-1), B_w1, B_b1, B_w2, B_b2)
    return h2
```

```cpp
#include <hip/hip_runtime.h>
#include <hip/hip_bf16.h>
typedef __hip_bfloat16 bf16;
constexpr int S=2048, D=576, H=32, DH=18, F=4608;
__device__ inline float b2f(bf16 x){return __bfloat162float(x);}
__device__ inline bf16 f2b(float x){return __float2bfloat16(x);}

__global__ void k_ln(const bf16* x, const float* add, float* hout, float* out, int twice){
  int r=blockIdx.x, t=threadIdx.x; float v[9]; float s=0;
  for(int i=0;i<9;i++){ float a=b2f(x[r*D+t+64*i]); if(add) a = b2f(f2b(a + b2f(f2b(add[r*D+t+64*i])))); v[i]=a; s+=a; if(hout) hout[r*D+t+64*i]=a;}
  for(int p=0;p<=twice;p++){
    s=0; for(int i=0;i<9;i++) s+=v[i];
    for(int o=32;o;o>>=1) s+=__shfl_xor(s,o);
    float m=s/D; float q=0; for(int i=0;i<9;i++){ float d=v[i]-m; q+=d*d;}
    for(int o=32;o;o>>=1) q+=__shfl_xor(q,o);
    float rs=rsqrtf(q/D+1e-5f);
    for(int i=0;i<9;i++) v[i]=b2f(f2b((v[i]-m)*rs));
  }
  for(int i=0;i<9;i++) out[r*D+t+64*i]=v[i];
}
__global__ void k_gemm(const float* A, long sam, long sak, const bf16* B, float* C, int M,int N,int K, const bf16* bias, int gelu){
  __shared__ float sa[16][17], sb[16][17];
  int tx=threadIdx.x&15, ty=threadIdx.x>>4; int m=blockIdx.y*16+ty, n=blockIdx.x*16+tx; float acc=0;
  for(int k0=0;k0<K;k0+=16){
    sa[ty][tx]=A[(long)m*sam+(long)(k0+tx)*sak]; sb[ty][tx]=b2f(B[(long)(k0+ty)*N+n]); __syncthreads();
    for(int k=0;k<16;k++) acc+=sa[ty][k]*sb[k][tx]; __syncthreads();
  }
  acc=b2f(f2b(acc));
  if(bias) acc=b2f(f2b(acc+b2f(bias[n])));
  if(gelu){ float u=acc; acc=0.5f*u*(1.f+tanhf(0.7978845608028654f*(u+0.044715f*u*u*u))); acc=b2f(f2b(acc)); }
  C[(long)m*N+n]=acc;
}
__global__ void k_attn(const float* qkv, float* o){
  int h=blockIdx.y; int q=blockIdx.x*64+threadIdx.x; float qv[DH], acc[DH];
  for(int d=0;d<DH;d++){qv[d]=qkv[(long)q*3*D+h*DH+d]*0.23570226039551584f; acc[d]=0;}
  float m=-1e30f,l=0;
  __shared__ float sk[64][DH], sv[64][DH];
  for(int k0=0;k0<S;k0+=64){
    __syncthreads();
    for(int d=0;d<DH;d++){ sk[threadIdx.x][d]=qkv[(long)(k0+threadIdx.x)*3*D+D+h*DH+d]; sv[threadIdx.x][d]=qkv[(long)(k0+threadIdx.x)*3*D+2*D+h*DH+d]; }
    __syncthreads();
    for(int j=0;j<64;j++){ float s=0; for(int d=0;d<DH;d++) s+=qv[d]*sk[j][d];
      float mn=fmaxf(m,s); float a=__expf(m-mn), p=__expf(s-mn); l=l*a+p; for(int d=0;d<DH;d++) acc[d]=acc[d]*a+p*sv[j][d]; m=mn; }
  }
  for(int d=0;d<DH;d++) o[(long)q*D+h*DH+d]=b2f(f2b(acc[d]/l));
}
__global__ void k_final(const float* h, const float* y, bf16* out){
  int s=blockIdx.x; for(int d=threadIdx.x; d<D; d+=64) out[s*D+d]=f2b(h[s*D+d]+y[(long)d*S+s]);
}
extern "C" void kernel_launch(void* const* d_in, const int* in_sizes, int n_in, void* d_out, int out_size, void* d_ws, size_t ws_size, hipStream_t stream){
  const bf16* x=(const bf16*)d_in[0]; const bf16* wqkv=(const bf16*)d_in[1]; const bf16* wo=(const bf16*)d_in[2];
  const bf16* w1=(const bf16*)d_in[4]; const bf16* b1=(const bf16*)d_in[5]; const bf16* w2=(const bf16*)d_in[6]; const bf16* b2=(const bf16*)d_in[7];
  float* ws=(float*)d_ws;
  float* xn=ws; float* qkv=xn+S*D; float* o=qkv+S*3*D; float* ao=o+S*D; float* h=ao+S*D; float* hh=h+S*D; float* u=hh+S*D; float* y=u+(long)D*F;
  k_ln<<<S,64,0,stream>>>(x,nullptr,nullptr,xn,0);
  k_gemm<<<dim3(3*D/16,S/16),256,0,stream>>>(xn,D,1,wqkv,qkv,S,3*D,D,nullptr,0);
  k_attn<<<dim3(S/64,H),64,0,stream>>>(qkv,o);
  k_gemm<<<dim3(D/16,S/16),256,0,stream>>>(o,D,1,wo,ao,S,D,D,nullptr,0);
  k_ln<<<S,64,0,stream>>>(x,ao,h,hh,1);
  k_gemm<<<dim3(F/16,D/16),256,0,stream>>>(hh,1,D,w1,u,D,F,S,b1,1);
  k_gemm<<<dim3(S/16,D/16),256,0,stream>>>(u,F,1,w2,y,D,S,F,b2,0);
  k_final<<<S,64,0,stream>>>(h,y,(bf16*)d_out);
}
```

```cpp
#include <hip/hip_runtime.h>
#include <stdint.h>

typedef unsigned short bf16_t;
typedef short bf16x8 __attribute__((ext_vector_type(8)));
typedef short s16x4 __attribute__((ext_vector_type(4)));
typedef float f32x2 __attribute__((ext_vector_type(2)));
typedef float f32x4 __attribute__((ext_vector_type(4)));
typedef float f32x16 __attribute__((ext_vector_type(16)));
typedef unsigned u32x2 __attribute__((ext_vector_type(2)));
typedef unsigned u32x4 __attribute__((ext_vector_type(4)));
typedef __bf16 bfx2 __attribute__((ext_vector_type(2)));
typedef __attribute__((address_space(1))) unsigned long long gu64;
typedef __attribute__((address_space(1))) unsigned gu32;
#define LAS __attribute__((address_space(3)))
#define DI __device__ __forceinline__
#define RLX_AGENT __ATOMIC_RELAXED, __HIP_MEMORY_SCOPE_AGENT

constexpr int S = 2048, D = 576, F = 4608;
constexpr int CHUNK = 18944, BLOB = 8 * CHUNK;
constexpr size_t WS_QB = 0;
constexpr size_t WS_QR = WS_QB + 32 * 64 * 1024;
constexpr size_t WS_KV = WS_QR + 32 * 2048 * 4;
constexpr size_t WS_OF = WS_KV + 32 * (size_t)BLOB;
constexpr size_t WS_HF = WS_OF + (size_t)S * D * 2;
constexpr size_t WS_HH = WS_HF + (size_t)S * D * 2;
constexpr size_t WS_UF = WS_HH + (size_t)S * D * 2;
constexpr size_t WS_ST = WS_UF + (size_t)D * F * 2;
constexpr size_t WS_FL = WS_ST + (size_t)S * 6 * 8;
constexpr size_t WS_PX = WS_FL + 256 * 128;
constexpr float EPS = 1e-5f;
constexpr float QSCALE = 0.23570226039551584f * 1.4426950408889634f;
constexpr unsigned SPIN_CAP = 2000000u;

DI unsigned rfl(unsigned x) { return __builtin_amdgcn_readfirstlane(x); }
DI __amdgpu_buffer_rsrc_t mksrd(const void* p, unsigned bytes) { return __builtin_amdgcn_make_buffer_rsrc((void*)p, (short)0, (int)bytes, 0x00020000); }
DI void dma16(unsigned ldsaddr, unsigned voff, __amdgpu_buffer_rsrc_t srd, unsigned soff) {
    asm volatile("s_mov_b32 m0, %0\n\ts_nop 0\n\tbuffer_load_dwordx4 %1, %2, %3 offen lds" ::"s"(ldsaddr), "v"(voff), "s"(srd), "s"(soff) : "memory");
}
DI s16x4 trrd(LAS const char* p) { return __builtin_amdgcn_ds_read_tr16_b64_v4i16((LAS s16x4*)p); }
DI bf16x8 rd128(LAS const char* p) { return *(LAS const bf16x8*)p; }
DI bf16x8 cat(s16x4 a, s16x4 b) { return __builtin_shufflevector(a, b, 0, 1, 2, 3, 4, 5, 6, 7); }
DI unsigned cvtpk(float lo, float hi) { unsigned r; asm("v_cvt_pk_bf16_f32 %0, %1, %2" : "=v"(r) : "v"(lo), "v"(hi)); return r; }
DI float blo(unsigned x) { return __uint_as_float(x << 16); }
DI float bhi(unsigned x) { return __uint_as_float(x & 0xffff0000u); }
DI float dot2(unsigned a, unsigned b, float c) { return __builtin_amdgcn_fdot2_f32_bf16(__builtin_bit_cast(bfx2, a), __builtin_bit_cast(bfx2, b), c, false); }
DI float swapadd32(float v) { auto r = __builtin_amdgcn_permlane32_swap(__float_as_uint(v), __float_as_uint(v), false, false); return __uint_as_float(r[0]) + __uint_as_float(r[1]); }
DI float swapmax32(float v) { auto r = __builtin_amdgcn_permlane32_swap(__float_as_uint(v), __float_as_uint(v), false, false); return __builtin_fmaxf(__uint_as_float(r[0]), __uint_as_float(r[1])); }
DI float swapadd16(float v) { auto r = __builtin_amdgcn_permlane16_swap(__float_as_uint(v), __float_as_uint(v), false, false); return __uint_as_float(r[0]) + __uint_as_float(r[1]); }
DI float gelu1(float u) {
    const float t = __builtin_fmaf(u * u, -2.f * 0.7978845608028654f * 0.044715f * 1.4426950408889634f, -2.f * 0.7978845608028654f * 1.4426950408889634f);
    return u * __builtin_amdgcn_rcpf(1.f + __builtin_amdgcn_exp2f(t * u));
}
#define MFMA16(a, b, c) __builtin_amdgcn_mfma_f32_16x16x32_bf16(a, b, c, 0, 0, 0)
#define MFMA32(a, b, c) __builtin_amdgcn_mfma_f32_32x32x16_bf16(a, b, c, 0, 0, 0)
#define MFMA32K8(a, b, c) __builtin_amdgcn_mfma_f32_32x32x8bf16_1k(a, b, c, 0, 0, 0)
#define BAR() asm volatile("s_barrier" ::: "memory")
#define WAITL0() asm volatile("s_waitcnt lgkmcnt(0)" ::: "memory")
#define SB() __builtin_amdgcn_sched_barrier(0)
#define SGB(m, n) __builtin_amdgcn_sched_group_barrier(m, n, 0)
#define UNR _Pragma("unroll")
template <int N> DI void waitv() { asm volatile("s_waitcnt vmcnt(%0)" ::"i"(N) : "memory"); }
#define Z8 ((bf16x8){0, 0, 0, 0, 0, 0, 0, 0})

template <int NT, int NS, int P, class Issue> DI void ring_loader(Issue issue) {
    issue(0, 0); issue(1, 1);
    waitv<P>(); BAR();
    UNR for (int t = 2; t < NS; t++) issue(t, t);
    int slot = 0;
    for (int t = 1; t <= NT - NS; t++) { waitv<P*(NS - 2)>(); BAR(); issue(t + NS - 1, slot); slot = (slot == NS - 1) ? 0 : slot + 1; }
    if constexpr (NS >= 5) { waitv<P * 3>(); BAR(); }
    if constexpr (NS >= 4) { waitv<P * 2>(); BAR(); }
    waitv<P>(); BAR();
    waitv<0>(); BAR();
}

constexpr int Q_STG = 32768, Q_NS = 4, Q_ST = Q_NS * Q_STG;
template <bool VT> DI void qkv_consumer(char* lds, char* ws, int mt, int nt, int wid, int lane) {
    const int wn = wid & 1, wsn = wid >> 1, r = lane & 31, h = lane >> 5, g = lane >> 4, q = (lane >> 2) & 3, p = lane & 3;
    unsigned xoff[2][2], woff[2];
    UNR for (int b = 0; b < 2; b++) { const int row = wsn * 64 + b * 32 + r; UNR for (int e = 0; e < 2; e++) xoff[b][e] = row * 128 + (((2 * e + h) ^ ((row >> 1) & 3)) | (((row >> 1) & 4))) * 16; }
    UNR for (int a = 0; a < 2; a++) woff[a] = 16384 + (8 * h + q) * 256 + ((((wn * 2 + a) * 2 + (g & 1)) ^ (q << 1)) * 32) + p * 8;
    f32x16 acc[2][2];
    UNR for (int a = 0; a < 2; a++) UNR for (int b = 0; b < 2; b++) acc[a][b] = (f32x16){};
    bf16x8 xw[2], xx[2], yw[2] = {Z8, Z8}, yx[2] = {Z8, Z8};
#define QRD(W, X, ks) do { W[0] = cat(trrd(base + woff[0] + (ks) * 4096), trrd(base + woff[0] + (ks) * 4096 + 1024)); X[0] = rd128(base + (xoff[0][(ks) & 1] ^ (((ks) >> 1) * 64))); \
        X[1] = rd128(base + (xoff[1][(ks) & 1] ^ (((ks) >> 1) * 64))); W[1] = cat(trrd(base + woff[1] + (ks) * 4096), trrd(base + woff[1] + (ks) * 4096 + 1024)); } while (0)
#define QMM(W, X) do { UNR for (int a = 0; a < 2; a++) UNR for (int b = 0; b < 2; b++) acc[a][b] = VT ? MFMA32(X[b], W[a], acc[a][b]) : MFMA32(W[a], X[b], acc[a][b]); } while (0)
#define QSG() do { UNR for (int i = 0; i < 3; i++) { SGB(0x8, 1); SGB(0x100, 2); } SGB(0x8, 4); } while (0)
    int slot = 0;
    for (int t = 0; t < 9; t++) {
        BAR(); LAS const char* base = (LAS const char*)lds + slot * Q_STG;
        SB(); QRD(xw, xx, 0); QMM(yw, yx); QSG(); SB(); QRD(yw, yx, 1); QMM(xw, xx); QSG();
        SB(); QRD(xw, xx, 2); QMM(yw, yx); QSG(); SB(); QRD(yw, yx, 3); QMM(xw, xx); QSG(); SB();
        WAITL0(); slot = (slot == Q_NS - 1) ? 0 : slot + 1;
    }
    QMM(yw, yx);
    BAR();
    LAS const float* mu = (LAS const float*)(lds + Q_ST); LAS const float* rs = mu + 128; LAS const float* cs = mu + 256;
    if constexpr (!VT) {
        float m_[2], r_[2];
        UNR for (int b = 0; b < 2; b++) { m_[b] = mu[wsn * 64 + b * 32 + r]; r_[b] = rs[wsn * 64 + b * 32 + r]; }
        UNR for (int a = 0; a < 2; a++) UNR for (int m = 0; m < 4; m++) {
            const int nl = wn * 64 + a * 32 + 8 * m + 4 * h, n = nt * 128 + nl; const f32x4 c4 = *(LAS const f32x4*)(cs + nl);
            const bool isk = n >= D; const int Qd = (n - (isk ? D : 0)) >> 2, ph = (Qd * 57) >> 9, qm = Qd - 9 * ph;
            const int head = 2 * ph + (qm > 4), pq = qm > 4 ? qm - 5 : qm;
            UNR for (int b = 0; b < 2; b++) {
                const int T = mt * 4 + wsn * 2 + b; const float rr = isk ? r_[b] : r_[b] * QSCALE;
                float v[4]; UNR for (int e = 0; e < 4; e++) v[e] = __builtin_fmaf(-m_[b], c4[e], acc[a][b][4 * m + e]) * rr;
                const u32x2 o = {cvtpk(v[0], v[1]), cvtpk(v[2], v[3])};
                if (qm != 4) {
                    const size_t tb = isk ? WS_KV + (size_t)head * BLOB + (T >> 3) * CHUNK + (T & 7) * 1024 : WS_QB + (size_t)(head * 64 + T) * 1024;
                    *(u32x2*)(ws + tb + (pq & 1) * 512 + (pq >> 1) * 256 + r * 8) = o;
                } else {
                    const size_t tb = isk ? WS_KV + (size_t)head * BLOB + (T >> 3) * CHUNK + 8192 + (T & 7) * 128 : WS_QR + (size_t)(head * 64 + T) * 128;
                    *(unsigned*)(ws + tb + r * 4) = o[0];
                    *(unsigned*)(ws + tb + (isk ? BLOB : 8192) + r * 4) = o[1];
                }
            }
        }
    } else {
        UNR for (int a = 0; a < 2; a++) {
            const int nl = wn * 64 + a * 32 + r, col = nt * 128 + nl - 2 * D; const float c1 = cs[nl];
            const int head = (col * 3641) >> 16, d = col - 18 * head;
            UNR for (int b = 0; b < 2; b++) {
                const int T = mt * 4 + wsn * 2 + b;
                char* tb = ws + WS_KV + (size_t)head * BLOB + (T >> 3) * CHUNK + 9216 + (T & 7) * 1216 + h * 304 + d * 16;
                UNR for (int s2 = 0; s2 < 2; s2++) {
                    u32x4 o;
                    UNR for (int e2 = 0; e2 < 2; e2++) {
                        const int m = 2 * s2 + e2, tl = wsn * 64 + b * 32 + 8 * m + 4 * h; const f32x4 m4 = *(LAS const f32x4*)(mu + tl), r4 = *(LAS const f32x4*)(rs + tl);
                        float v[4]; UNR for (int e = 0; e < 4; e++) v[e] = __builtin_fmaf(-m4[e], c1, acc[a][b][4 * m + e]) * r4[e];
                        o[2 * e2] = cvtpk(v[0], v[1]); o[2 * e2 + 1] = cvtpk(v[2], v[3]);
                    }
                    if (col < D) { *(u32x4*)(tb + s2 * 608) = o; if (d == 17) *(u32x4*)(tb + s2 * 608 + 16) = (u32x4){0x3f803f80u, 0x3f803f80u, 0x3f803f80u, 0x3f803f80u}; }
                }
            }
        }
    }
#undef QRD
#undef QMM
#undef QSG
}
__global__ __launch_bounds__(768, 3) void k_qkv(const bf16_t* __restrict__ x, const bf16_t* __restrict__ w, char* __restrict__ ws) {
    __shared__ __attribute__((aligned(16))) char lds[Q_ST + 1536];
    const int tid = threadIdx.x, lane = tid & 63, wid = rfl(tid >> 6), bid = blockIdx.x;
    if (bid >= 224) {
        const int i = (bid - 224) * 768 + tid;
        if (i < S * 6 * 8 / 16) *(u32x4*)(ws + WS_ST + (size_t)i * 16) = (u32x4){~0u, ~0u, ~0u, ~0u};
        if (i < 256 * 128 / 16) *(u32x4*)(ws + WS_FL + (size_t)i * 16) = (u32x4){0u, 0u, 0u, 0u};
        return;
    }
    const int xg = bid & 7, idx = bid >> 3, mt = (xg >> 1) * 4 + (idx & 3), nt = (xg & 1) * 7 + (idx >> 2);
    const unsigned lds0 = (unsigned)(uintptr_t)lds;
    if (wid < 4) { if (nt >= 9) qkv_consumer<true>(lds, ws, mt, nt, wid, lane); else qkv_consumer<false>(lds, ws, mt, nt, wid, lane); }
    else if (wid < 8) {
        const int lw = wid - 4; const __amdgpu_buffer_rsrc_t sx = mksrd(x, S * D * 2), sw = mksrd(w, D * 3 * D * 2);
        unsigned vx[2];
        UNR for (int e = 0; e < 2; e++) vx[e] = (mt * 128 + 32 * lw + (lane >> 3)) * 1152 + ((lane & 7) ^ (4 * e + (lane >> 4))) * 16;
        const int rq = lane >> 4, c = lane & 15; const unsigned vw = (16 * lw + rq) * 3456 + nt * 256 + ((((c >> 1) ^ (rq << 1)) * 2) + (c & 1)) * 16;
        ring_loader<9, Q_NS, 8>([&](int t, int slot) {
            const unsigned lb = lds0 + slot * Q_STG;
            UNR for (int i = 0; i < 4; i++) dma16(lb + 16384 + (16 * lw + 4 * i) * 256, vw, sw, (t * 64 + 4 * i) * 3456);
            UNR for (int i = 0; i < 4; i++) dma16(lb + (32 * lw + 8 * i) * 128, vx[i & 1], sx, 8 * i * 1152 + t * 128);
        });
        BAR();
    } else {
        const int sw = wid - 8, r = lane & 31, h = lane >> 5, g = lane >> 4, q = (lane >> 2) & 3, p = lane & 3, row = sw * 32 + r;
        const unsigned woff = 16384 + (8 * h + q) * 256 + (((sw * 2 + (g & 1)) ^ (q << 1)) * 32) + p * 8;
        float s1 = 0.f, s2 = 0.f, c1 = 0.f; int slot = 0;
        for (int t = 0; t < 9; t++) {
            BAR(); LAS const char* base = (LAS const char*)lds + slot * Q_STG;
            UNR for (int ks = 0; ks < 4; ks++) {
                const u32x4 xv = *(LAS const u32x4*)(base + row * 128 + (((2 * ks + h) ^ ((row >> 1) & 7)) * 16));
                const u32x2 w0 = __builtin_bit_cast(u32x2, trrd(base + woff + ks * 4096)), w1 = __builtin_bit_cast(u32x2, trrd(base + woff + ks * 4096 + 1024));
                UNR for (int e = 0; e < 4; e++) { s1 = dot2(xv[e], 0x3f803f80u, s1); s2 = dot2(xv[e], xv[e], s2); }
                c1 = dot2(w0[0], 0x3f803f80u, c1); c1 = dot2(w0[1], 0x3f803f80u, c1); c1 = dot2(w1[0], 0x3f803f80u, c1); c1 = dot2(w1[1], 0x3f803f80u, c1);
            }
            WAITL0(); slot = (slot == Q_NS - 1) ? 0 : slot + 1;
        }
        s1 = swapadd32(s1); s2 = swapadd32(s2); c1 = swapadd32(c1);
        const float m = s1 * (1.f / D), var = __builtin_fmaxf(__builtin_fmaf(-m, m, s2 * (1.f / D)), 0.f);
        LAS float* st = (LAS float*)(lds + Q_ST);
        if (h == 0) { st[row] = m; st[128 + row] = __builtin_amdgcn_rsqf(var + EPS); st[256 + row] = c1; }
        WAITL0(); BAR();
    }
}

#define AT_IT(SC, SN, c0, c1, c2, c3, c4, c5, c6, c7, c8, c9, c10, c11, c12, c13, c14, c15, KO0, KO1, KR, V1, V0) \
    "s_waitcnt lgkmcnt(2)\n v_mfma_f32_32x32x16_bf16 " SN ", v[56:59], v[70:73], 0\n ds_read2st64_b64 v[56:59], v76 offset0:" KO0 " offset1:" KO1 "\n" \
    "v_exp_f32 " c0 "," c0 "\n v_exp_f32 " c1 "," c1 "\n v_exp_f32 " c2 "," c2 "\n v_exp_f32 " c3 "," c3 "\n v_cvt_pk_bf16_f32 v48," c0 "," c1 "\n v_cvt_pk_bf16_f32 v49," c2 "," c3 "\n" \
    "v_mfma_f32_32x32x8_bf16 " SN ", v[60:61], v[74:75], " SN "\n ds_read_b32 v60, v77 offset:" KR "\n" \
    "v_exp_f32 " c4 "," c4 "\n v_exp_f32 " c5 "," c5 "\n v_exp_f32 " c6 "," c6 "\n v_exp_f32 " c7 "," c7 "\n v_cvt_pk_bf16_f32 v50," c4 "," c5 "\n v_cvt_pk_bf16_f32 v51," c6 "," c7 "\n" \
    "s_waitcnt lgkmcnt(2)\n v_mfma_f32_32x32x16_bf16 v[32:47], v[66:69], v[52:55], v[32:47]\n" \
    "v_exp_f32 " c8 "," c8 "\n v_exp_f32 " c9 "," c9 "\n v_exp_f32 " c10 "," c10 "\n v_exp_f32 " c11 "," c11 "\n v_cvt_pk_bf16_f32 v52," c8 "," c9 "\n v_cvt_pk_bf16_f32 v53," c10 "," c11 "\n" \
    "v_mfma_f32_32x32x16_bf16 v[32:47], v[62:65], v[48:51], v[32:47]\n" \
    "s_mov_b64 exec, s[36:37]\n ds_read_b128 v[66:69], v78 offset:" V1 "\n ds_read_b128 v[62:65], v78 offset:" V0 "\n s_mov_b64 exec, -1\n" \
    "v_exp_f32 " c12 "," c12 "\n v_exp_f32 " c13 "," c13 "\n v_exp_f32 " c14 "," c14 "\n v_exp_f32 " c15 "," c15 "\n v_cvt_pk_bf16_f32 v54," c12 "," c13 "\n v_cvt_pk_bf16_f32 v55," c14 "," c15 "\n"
#define AT_A(KO0, KO1, KR, V1, V0) AT_IT("v[0:15]", "v[16:31]", "v0", "v1", "v2", "v3", "v4", "v5", "v6", "v7", "v8", "v9", "v10", "v11", "v12", "v13", "v14", "v15", KO0, KO1, KR, V1, V0)
#define AT_B(KO0, KO1, KR, V1, V0) AT_IT("v[16:31]", "v[0:15]", "v16", "v17", "v18", "v19", "v20", "v21", "v22", "v23", "v24", "v25", "v26", "v27", "v28", "v29", "v30", "v31", KO0, KO1, KR, V1, V0)
#define AT_SWAP 6
__global__ __launch_bounds__(576, 3) void k_attn(char* __restrict__ ws) {
    __shared__ __attribute__((aligned(16))) char lds[BLOB + 10240];
    const int tid = threadIdx.x, lane = tid & 63, wid = rfl(tid >> 6), bid = blockIdx.x;
    const int head = (bid & 7) * 4 + ((bid >> 3) & 3), qp = bid >> 5;
    const unsigned lds0 = (unsigned)(uintptr_t)lds;
    const __amdgpu_buffer_rsrc_t skv = mksrd(ws + WS_KV + (size_t)head * BLOB, BLOB);
    const unsigned v16 = lane * 16;
    if (wid == 8) {
        for (int pc = 19; pc < 60; pc++) dma16(lds0 + pc * 1024, v16, skv, pc * 1024);
        BAR();
        for (int pc = 60; pc < 148; pc += 4) { waitv<40>(); UNR for (int i = 0; i < 4; i++) dma16(lds0 + (pc + i) * 1024, v16, skv, (pc + i) * 1024); }
        waitv<0>(); BAR();
        return;
    }
    dma16(lds0 + wid * 1024, v16, skv, wid * 1024); dma16(lds0 + (wid + 8) * 1024, v16, skv, (wid + 8) * 1024);
    if (wid < 3) dma16(lds0 + (wid + 16) * 1024, v16, skv, (wid + 16) * 1024);
    const int r = lane & 31, h = lane >> 5, T = qp * 8 + wid;
    const char* qb = ws + WS_QB + (size_t)(head * 64 + T) * 1024 + h * 256 + r * 8;
    const u32x2 q0 = *(const u32x2*)qb, q1 = *(const u32x2*)(qb + 512);
    const unsigned qrest = *(const unsigned*)(ws + WS_QR + (size_t)(head * 64 + T) * 128 + r * 4);
    const bf16x8 qm = __builtin_bit_cast(bf16x8, (u32x4){q0[0], q0[1], q1[0], q1[1]});
    u32x2 qr = {h ? 0u : qrest, 0u};
    waitv<0>(); BAR();
    const unsigned ka = h * 256 + r * 8, kra = 8192 + r * 4, va = 9216 + h * 304 + r * 16;
    {
        LAS const char* b = (LAS const char*)lds;
        const u32x2 k0 = *(LAS const u32x2*)(b + ka), k1 = *(LAS const u32x2*)(b + ka + 512); const u32x2 kr = {*(LAS const unsigned*)(b + kra), 0u};
        f32x16 s = MFMA32(__builtin_bit_cast(bf16x8, (u32x4){k0[0], k0[1], k1[0], k1[1]}), qm, (f32x16){});
        s = MFMA32K8(__builtin_bit_cast(s16x4, kr), __builtin_bit_cast(s16x4, qr), s);
        float m = s[0]; UNR for (int i = 1; i < 16; i++) m = __builtin_fmaxf(m, s[i]);
        m = swapmax32(m);
        qr[1] = h ? 0u : (cvtpk(-m, 0.f) & 0xffffu);
    }
    f32x16 O;
    {
        unsigned a0 = lds0 + ka, a1 = lds0 + kra, a2 = lds0 + va;
        asm volatile(
            "s_mov_b32 s36, 0x7ffff\n s_mov_b32 s37, 0x7ffff\n s_mov_b32 s38, 0\n"
            "s_setprio 1\n s_cmp_ge_u32 %[wid], 4\n s_cbranch_scc1 1f\n s_setprio 2\n 1:\n"
            "ds_read2st64_b64 v[56:59], v76 offset0:0 offset1:1\n ds_read_b32 v60, v77\n"
            "v_mov_b32 v61, 0x3f80\n"
            "v_mov_b32 v32, 0\n v_mov_b32 v33, 0\n v_mov_b32 v34, 0\n v_mov_b32 v35, 0\n v_mov_b32 v36, 0\n v_mov_b32 v37, 0\n v_mov_b32 v38, 0\n v_mov_b32 v39, 0\n"
            "v_mov_b32 v40, 0\n v_mov_b32 v41, 0\n v_mov_b32 v42, 0\n v_mov_b32 v43, 0\n v_mov_b32 v44, 0\n v_mov_b32 v45, 0\n v_mov_b32 v46, 0\n v_mov_b32 v47, 0\n"
            "v_mov_b32 v52, 0\n v_mov_b32 v53, 0\n v_mov_b32 v54, 0\n v_mov_b32 v55, 0\n"
            "v_mov_b32 v62, 0\n v_mov_b32 v63, 0\n v_mov_b32 v64, 0\n v_mov_b32 v65, 0\n v_mov_b32 v66, 0\n v_mov_b32 v67, 0\n v_mov_b32 v68, 0\n v_mov_b32 v69, 0\n"
            "s_waitcnt lgkmcnt(0)\n"
            "v_mfma_f32_32x32x16_bf16 v[0:15], v[56:59], v[70:73], 0\n v_mfma_f32_32x32x8_bf16 v[0:15], v[60:61], v[74:75], v[0:15]\n"
            "ds_read2st64_b64 v[56:59], v76 offset0:2 offset1:3\n ds_read_b32 v60, v77 offset:128\n"
            "s_mov_b64 exec, s[36:37]\n ds_read_b128 v[66:69], v78 offset:608\n ds_read_b128 v[62:65], v78\n s_mov_b64 exec, -1\n"
            "s_nop 7\n s_nop 7\n"
            "2:\n"
            AT_A("4", "5", "256", "608", "1216") AT_B("6", "7", "384", "1824", "2432") AT_A("8", "9", "512", "3040", "3648") AT_B("10", "11", "640", "4256", "4864")
            AT_A("12", "13", "768", "5472", "6080") AT_B("14", "15", "896", "6688", "7296")
            "s_cmp_lg_u32 s38, 0\n s_cbranch_scc1 3f\n s_barrier\n 3:\n"
            AT_A("37", "38", "18944", "7904", "8512") AT_B("39", "40", "19072", "9120", "18944")
            "v_add_u32 v76, 0x4a00, v76\n v_add_u32 v77, 0x4a00, v77\n v_add_u32 v78, 0x4a00, v78\n"
            "s_add_u32 s38, s38, 1\n s_cmp_lg_u32 s38, %[swap]\n s_cbranch_scc1 4f\n s_cmp_ge_u32 %[wid], 4\n s_cbranch_scc1 4f\n s_setprio 0\n 4:\n"
            "s_cmp_lt_u32 s38, 8\n s_cbranch_scc1 2b\n"
            "s_waitcnt lgkmcnt(0)\n v_mfma_f32_32x32x16_bf16 v[32:47], v[66:69], v[52:55], v[32:47]\n s_setprio 0\n s_nop 7\n s_nop 7\n"
            : "={v[32:47]}"(O), "+{v76}"(a0), "+{v77}"(a1), "+{v78}"(a2)
            : "{v[70:73]}"(qm), "{v[74:75]}"(qr), [wid] "s"(wid), [swap] "i"(AT_SWAP)
            : "memory", "scc", "s36", "s37", "s38", "v0", "v1", "v2", "v3", "v4", "v5", "v6", "v7", "v8", "v9", "v10", "v11", "v12", "v13", "v14", "v15", "v16", "v17", "v18", "v19", "v20", "v21", "v22", "v23",
              "v24", "v25", "v26", "v27", "v28", "v29", "v30", "v31", "v48", "v49", "v50", "v51", "v52", "v53", "v54", "v55", "v56", "v57", "v58", "v59", "v60", "v61", "v62", "v63", "v64", "v65", "v66", "v67", "v68", "v69");
    }
    float l = __shfl(O[10], r);
    const bool bad = !(l < 1.8e19f && l > 1e-18f);
    if (__builtin_expect(__any(bad), 0)) {
        LAS const char* b = (LAS const char*)lds; qr[1] = 0u; float m = -INFINITY;
        const unsigned vs = 9216 + h * 304 + (r < 19 ? r : r - 16) * 16;
        for (int pass = 0; pass < 2; pass++) {
            O = (f32x16){};
            for (int t = 0; t < 64; t++) {
                LAS const char* cb = b + (t >> 3) * CHUNK; const int i = t & 7;
                const u32x2 k0 = *(LAS const u32x2*)(cb + i * 1024 + ka), k1 = *(LAS const u32x2*)(cb + i * 1024 + ka + 512); const u32x2 kr = {*(LAS const unsigned*)(cb + i * 128 + kra), 0u};
                f32x16 s = MFMA32(__builtin_bit_cast(bf16x8, (u32x4){k0[0], k0[1], k1[0], k1[1]}), qm, (f32x16){});
                s = MFMA32K8(__builtin_bit_cast(s16x4, kr), __builtin_bit_cast(s16x4, qr), s);
                if (pass == 0) { UNR for (int e = 0; e < 16; e++) m = __builtin_fmaxf(m, s[e]); }
                else {
                    u32x4 p0, p1;
                    UNR for (int e = 0; e < 4; e++) { p0[e] = cvtpk(__builtin_amdgcn_exp2f(s[2 * e] - m), __builtin_amdgcn_exp2f(s[2 * e + 1] - m)); p1[e] = cvtpk(__builtin_amdgcn_exp2f(s[8 + 2 * e] - m), __builtin_amdgcn_exp2f(s[9 + 2 * e] - m)); }
                    asm volatile("s_nop 1" : "+v"(p0), "+v"(p1));
                    O = MFMA32(rd128(cb + i * 1216 + vs), __builtin_bit_cast(bf16x8, p0), O); O = MFMA32(rd128(cb + i * 1216 + 608 + vs), __builtin_bit_cast(bf16x8, p1), O);
                }
            }
            m = swapmax32(m);
        }
        l = __shfl(O[10], r);
    }
    const float il = 1.f / l; const int qrow = T * 32 + r;
    char* of = ws + WS_OF + (size_t)(qrow >> 4) * 18432;
    *(u32x4*)(of + (head >> 1) * 1024 + ((2 * (head & 1) + h) * 16 + (r & 15)) * 16) = (u32x4){cvtpk(O[0] * il, O[1] * il), cvtpk(O[2] * il, O[3] * il), cvtpk(O[4] * il, O[5] * il), cvtpk(O[6] * il, O[7] * il)};
    if (h == 0) *(unsigned*)(of + (16 + (head >> 4)) * 1024 + (((head & 15) >> 2) * 16 + (r & 15)) * 16 + (head & 3) * 4) = cvtpk(O[8] * il, O[9] * il);
}

constexpr int O_X = 9 * 12288, O_T = O_X + 12288;
__global__ __launch_bounds__(512, 2) void k_wo(const bf16_t* __restrict__ x, const bf16_t* __restrict__ wo, char* __restrict__ ws) {
    __shared__ __attribute__((aligned(16))) char lds[O_T + 4 * 3328];
    const int tid = threadIdx.x, lane = tid & 63, wid = rfl(tid >> 6), bid = blockIdx.x;
    const int idx = bid >> 3, st = (bid & 7) * 4 + idx / 6, nt = idx % 6;
    const unsigned lds0 = (unsigned)(uintptr_t)lds;
    if (wid >= 4) {
        const int lw = wid - 4; const __amdgpu_buffer_rsrc_t sw = mksrd(wo, D * D * 2), sx = mksrd(x, S * D * 2);
        unsigned vm[3], vr[3], vx[3];
        UNR for (int i = 0; i < 3; i++) {
            const int c = i * 64 + lane, rl = c / 12, cc = c - 12 * rl, rho = 16 * lw + rl, g = (rho >> 2) & 3, j = (rho & 3) + 4 * ((rho >> 4) & 1), kb = rho >> 5;
            const int col = nt * 192 + ((((cc >> 1) ^ (g & 1)) * 2) + (cc & 1)) * 16;
            vm[i] = (36 * kb + 18 * (g >> 1) + 4 * (g & 1) + (j & 3) + 8 * (j >> 2)) * 1152 + col;
            vr[i] = (18 * (16 * kb + 4 * g + (j >> 1)) + 16 + (j & 1)) * 1152 + col;
            vx[i] = (st * 64 + rho) * 1152 + nt * 192 + cc * 16;
        }
        auto issue = [&](int t) {
            UNR for (int i = 0; i < 3; i++) {
                if (t < 8) dma16(lds0 + t * 12288 + (3 * lw + i) * 1024, vm[i], sw, t * 72 * 1152);
                else if (t == 8) dma16(lds0 + t * 12288 + (3 * lw + i) * 1024, vr[i], sw, 0);
                else dma16(lds0 + O_X + (3 * lw + i) * 1024, vx[i], sx, 0);
            } };
        issue(0); issue(1); waitv<3>(); BAR(); issue(2); issue(3); issue(4);
        UNR for (int t = 1; t <= 5; t++) { waitv<9>(); BAR(); issue(t + 4); }
        waitv<9>(); BAR(); waitv<6>(); BAR(); waitv<3>(); BAR(); waitv<0>(); BAR();
        return;
    }
    const int s = lane & 15, g = lane >> 4, q = (lane >> 2) & 3, p = lane & 3, s16 = st * 4 + wid;
    bf16x8 of[18];
    UNR for (int b = 0; b < 18; b++) of[b] = *(const bf16x8*)(ws + WS_OF + (size_t)s16 * 18432 + b * 1024 + lane * 16);
    const unsigned be = (4 * g + q) * 192 + (g & 1) * 32 + 8 * p, bo = (4 * g + q) * 192 - (g & 1) * 32 + 8 * p;
    f32x4 acc[6];
    UNR for (int t = 0; t < 6; t++) acc[t] = (f32x4){0.f, 0.f, 0.f, 0.f};
    UNR for (int t = 0; t < 9; t++) {
        BAR(); LAS const char* base = (LAS const char*)lds + t * 12288;
        UNR for (int kb = 0; kb < 2; kb++) UNR for (int n = 0; n < 6; n++) {
            LAS const char* a = base + kb * 6144 + ((n & 1) ? bo : be) + n * 32;
            acc[n] = MFMA16(cat(trrd(a), trrd(a + 3072)), of[2 * t + kb], acc[n]);
        }
    }
    BAR();
    float hv[6][4], s1 = 0.f, s2 = 0.f;
    char* hf = ws + WS_HF + (size_t)s16 * 18432;
    UNR for (int n = 0; n < 6; n++) {
        const u32x2 xv = *(LAS const u32x2*)((LAS const char*)lds + O_X + (wid * 16 + s) * 192 + n * 32 + g * 8);
        const unsigned a0 = cvtpk(acc[n][0], acc[n][1]), a1 = cvtpk(acc[n][2], acc[n][3]);
        const u32x2 o = {cvtpk(blo(xv[0]) + blo(a0), bhi(xv[0]) + bhi(a0)), cvtpk(blo(xv[1]) + blo(a1), bhi(xv[1]) + bhi(a1))};
        hv[n][0] = blo(o[0]); hv[n][1] = bhi(o[0]); hv[n][2] = blo(o[1]); hv[n][3] = bhi(o[1]);
        UNR for (int e = 0; e < 4; e++) { s1 += hv[n][e]; s2 = __builtin_fmaf(hv[n][e], hv[n][e], s2); }
        const int t16 = nt * 6 + n, dT = t16 / 9, tl = t16 - 9 * dT;
        const unsigned off = tl < 8 ? (tl >> 1) * 1024 + ((2 * (tl & 1) + (g >> 1)) * 16 + s) * 16 + (g & 1) * 8 : 4096 + lane * 8;
        *(u32x2*)(hf + dT * 4608 + off) = o;
    }
    s1 = swapadd32(swapadd16(s1)); s2 = swapadd32(swapadd16(s2));
    gu64* gs = (gu64*)(ws + WS_ST) + (size_t)(s16 * 16 + s) * 6;
    { unsigned b2 = __float_as_uint(s2); if (b2 == ~0u) b2 = 0x7fc00000u;
      if (g == 0) __hip_atomic_store(gs + nt, ((unsigned long long)b2 << 32) | __float_as_uint(s1), RLX_AGENT); }
    unsigned long long w0, w1 = 0;
    for (unsigned spins = 0;; spins++) {
        w0 = __hip_atomic_load(gs + g, RLX_AGENT); if (g < 2) w1 = __hip_atomic_load(gs + g + 4, RLX_AGENT);
        if (__all((unsigned)(w0 >> 32) != ~0u && (unsigned)(w1 >> 32) != ~0u) || spins > SPIN_CAP) break;
        __builtin_amdgcn_s_sleep(1);
    }
    const float t1 = swapadd32(swapadd16(__uint_as_float((unsigned)w0) + __uint_as_float((unsigned)w1)));
    const float t2 = swapadd32(swapadd16(__uint_as_float((unsigned)(w0 >> 32)) + __uint_as_float((unsigned)(w1 >> 32))));
    const float m = t1 * (1.f / D), var = __builtin_fmaxf(__builtin_fmaf(-m, m, t2 * (1.f / D)), 0.f);
    const float r1 = __builtin_amdgcn_rsqf(var + EPS), rr = r1 * __builtin_amdgcn_rsqf(var * r1 * r1 + EPS);
    LAS char* tb = (LAS char*)lds + O_T + wid * 3328;
    UNR for (int n = 0; n < 6; n++) *(LAS u32x2*)(tb + s * 208 + n * 32 + g * 8) = (u32x2){cvtpk((hv[n][0] - m) * rr, (hv[n][1] - m) * rr), cvtpk((hv[n][2] - m) * rr, (hv[n][3] - m) * rr)};
    WAITL0();
    UNR for (int dt = 0; dt < 3; dt++) {
        LAS const char* a = tb + (4 * (g >> 1) + q) * 208 + dt * 64 + (g & 1) * 32 + p * 8;
        *(bf16x8*)(ws + WS_HH + (size_t)((nt * 128 + s16) * 3 + dt) * 1024 + lane * 16) = cat(trrd(a), trrd(a + 8 * 208));
    }
}

constexpr int A_STG = 28 * 1024, A_NS = 5;
__global__ __launch_bounds__(512, 2) void k_up(const bf16_t* __restrict__ w1, const bf16_t* __restrict__ b1, char* __restrict__ ws) {
    __shared__ __attribute__((aligned(16))) char lds[A_NS * A_STG];
    const int tid = threadIdx.x, lane = tid & 63, wid = rfl(tid >> 6), bid = blockIdx.x;
    const int L = (bid & 7) * 27 + (bid >> 3), ft = L / 6, dT = L % 6;
    const unsigned lds0 = (unsigned)(uintptr_t)lds;
    if (wid >= 4) {
        const int lw = wid - 4, rq = lane >> 4, c = lane & 15; const __amdgpu_buffer_rsrc_t sw = mksrd(w1, S * F * 2), sh = mksrd(ws + WS_HH, S * D * 2);
        const unsigned vw = (16 * lw + rq) * (F * 2) + ft * 256 + ((((c >> 1) ^ (rq << 1)) * 2) + (c & 1)) * 16, vh = lane * 16;
        ring_loader<32, A_NS, 7>([&](int t, int slot) {
            const unsigned lb = lds0 + slot * A_STG;
            UNR for (int i = 0; i < 4; i++) dma16(lb + (16 * lw + 4 * i) * 256, vw, sw, (t * 64 + 4 * i) * (F * 2));
            UNR for (int j = 0; j < 3; j++) dma16(lb + 16384 + (lw * 3 + j) * 1024, vh, sh, (dT * 32 + t) * 12288 + (lw * 3 + j) * 1024);
        });
        BAR(); BAR();
        return;
    }
    const int wf = wid & 1, kh = wid >> 1, r = lane & 31, h = lane >> 5, g = lane >> 4, q = (lane >> 2) & 3, p = lane & 3;
    const int f0 = ft * 128 + (wf * 2 + kh) * 32;
    u32x2 bb[4];
    UNR for (int m = 0; m < 4; m++) bb[m] = *(const u32x2*)(b1 + f0 + 8 * m + 4 * h);
    unsigned aoff[2];
    UNR for (int i = 0; i < 2; i++) aoff[i] = kh * 8192 + (4 * h + q) * 256 + ((((wf * 2 + i) * 2 + (g & 1)) ^ (q << 1)) * 32) + p * 8;
    const unsigned boff = 16384 + kh * 6144 + lane * 16;
    f32x16 acc[2][3];
    UNR for (int i = 0; i < 2; i++) UNR for (int j = 0; j < 3; j++) acc[i][j] = (f32x16){};
    bf16x8 xa[2], xb[3], ya[2] = {Z8, Z8}, yb[3] = {Z8, Z8, Z8};
#define ARD(A, B, ks) do { B[0] = rd128(base + boff + (ks) * 3072); A[0] = cat(trrd(base + aoff[0] + (ks) * 4096), trrd(base + aoff[0] + (ks) * 4096 + 2048)); B[1] = rd128(base + boff + (ks) * 3072 + 1024); \
        B[2] = rd128(base + boff + (ks) * 3072 + 2048); A[1] = cat(trrd(base + aoff[1] + (ks) * 4096), trrd(base + aoff[1] + (ks) * 4096 + 2048)); } while (0)
#define AMM(A, B) do { UNR for (int i = 0; i < 2; i++) UNR for (int j = 0; j < 3; j++) acc[i][j] = MFMA32(A[i], B[j], acc[i][j]); } while (0)
#define ASG() do { UNR for (int i = 0; i < 3; i++) { SGB(0x8, 1); SGB(0x100, 3); } SGB(0x8, 6); } while (0)
    int slot = 0;
    for (int t = 0; t < 32; t++) {
        BAR(); LAS const char* base = (LAS const char*)lds + slot * A_STG;
        SB(); ARD(xa, xb, 0); AMM(ya, yb); ASG(); SB(); ARD(ya, yb, 1); AMM(xa, xb); ASG(); SB();
        WAITL0(); slot = (slot == A_NS - 1) ? 0 : slot + 1;
    }
    AMM(ya, yb);
    BAR();
    LAS f32x4* ex = (LAS f32x4*)lds + wid * 12 * 64 + lane;
    UNR for (int j = 0; j < 3; j++) UNR for (int m = 0; m < 4; m++) {
        f32x4 v; UNR for (int e = 0; e < 4; e++) v[e] = kh ? acc[0][j][4 * m + e] : acc[1][j][4 * m + e];
        ex[(j * 4 + m) * 64] = v; }
    WAITL0(); BAR();
    LAS const f32x4* ey = (LAS const f32x4*)lds + (wid ^ 2) * 12 * 64 + lane;
    UNR for (int j = 0; j < 3; j++) {
        const int d = dT * 96 + j * 32 + r, dT9 = d / 144, dl = d - 144 * dT9;
        const int tl = dl < 128 ? 2 * (dl >> 5) + ((dl >> 2) & 1) : 8, rho = dl < 128 ? 4 * ((dl >> 3) & 3) + (dl & 3) : dl - 128;
        char* ub = ws + WS_UF + (size_t)((dT9 * 144 + (f0 >> 5)) * 9 + tl) * 1024 + (h * 16 + rho) * 16;
        UNR for (int s2 = 0; s2 < 2; s2++) {
            u32x4 o;
            UNR for (int e2 = 0; e2 < 2; e2++) {
                const int m = 2 * s2 + e2; const f32x4 y = ey[(j * 4 + m) * 64]; float v[4];
                UNR for (int e = 0; e < 4; e++) v[e] = y[e] + (kh ? acc[1][j][4 * m + e] : acc[0][j][4 * m + e]);
                o[2 * e2] = cvtpk(gelu1(v[0] + blo(bb[m][0])), gelu1(v[1] + bhi(bb[m][0]))); o[2 * e2 + 1] = cvtpk(gelu1(v[2] + blo(bb[m][1])), gelu1(v[3] + bhi(bb[m][1])));
            }
            *(u32x4*)(ub + s2 * 512) = o;
        }
    }
#undef ARD
#undef AMM
#undef ASG
}

constexpr int B_STG = 34 * 1024, B_NS = 4;
__global__ __launch_bounds__(512, 2) void k_down(const bf16_t* __restrict__ w2, const bf16_t* __restrict__ b2, char* __restrict__ ws, bf16_t* __restrict__ out) {
    __shared__ __attribute__((aligned(16))) char lds[B_NS * B_STG];
    const int tid = threadIdx.x, lane = tid & 63, wid = rfl(tid >> 6), bid = blockIdx.x;
    const int xg = bid & 7, idx = bid >> 3, kq = xg >> 1, st = (xg & 1) * 8 + (idx >> 2), dT = idx & 3, tile = st * 4 + dT;
    const unsigned lds0 = (unsigned)(uintptr_t)lds;
    if (wid >= 4) {
        const int lw = wid - 4, rq = lane >> 4, c = lane & 15; const __amdgpu_buffer_rsrc_t sw = mksrd(w2, S * F * 2), su = mksrd(ws + WS_UF, D * F * 2);
        unsigned vw[2];
        UNR for (int e = 0; e < 2; e++) vw[e] = (kq * 1152 + 16 * lw + rq) * (S * 2) + st * 256 + ((((c >> 1) ^ (4 * e + rq)) * 2) + (c & 1)) * 16;
        const unsigned vu = lane * 16;
        const int u0 = lw < 2 ? lw * 5 : 10 + (lw - 2) * 4, ul = lw < 2 ? 4 : 3;
        ring_loader<18, B_NS, 9>([&](int t, int slot) {
            const unsigned lb = lds0 + slot * B_STG;
            UNR for (int i = 0; i < 4; i++) dma16(lb + (16 * lw + 4 * i) * 256, vw[i & 1], sw, (t * 64 + 4 * i) * (S * 2));
            UNR for (int j = 0; j < 5; j++) { const int pc = u0 + (j < 4 ? j : ul); dma16(lb + 16384 + pc * 1024, vu, su, (dT * 144 + kq * 36 + 2 * t) * 9216 + pc * 1024); }
        });
        return;
    }
    const int s = lane & 15, g = lane >> 4, q = (lane >> 2) & 3, p = lane & 3, r7 = 4 * (g & 1) + q;
    unsigned boff[2];
    UNR for (int j = 0; j < 2; j++) boff[j] = (16 * (g >> 1) + r7) * 256 + (((wid * 2 + j) ^ r7) * 32) + p * 8;
    const unsigned aoff = 16384 + lane * 16;
    f32x4 acc[9][2];
    UNR for (int i = 0; i < 9; i++) UNR for (int j = 0; j < 2; j++) acc[i][j] = (f32x4){0.f, 0.f, 0.f, 0.f};
    bf16x8 xa[9], xb[2], ya[9], yb[2] = {Z8, Z8};
    UNR for (int i = 0; i < 9; i++) ya[i] = Z8;
#define BRD(A, B, ks) do { B[0] = cat(trrd(base + boff[0] + (ks) * 8192), trrd(base + boff[0] + (ks) * 8192 + 2048)); A[0] = rd128(base + aoff + (ks) * 9216); \
        B[1] = cat(trrd(base + boff[1] + (ks) * 8192), trrd(base + boff[1] + (ks) * 8192 + 2048)); UNR for (int i = 1; i < 9; i++) A[i] = rd128(base + aoff + (ks) * 9216 + i * 1024); } while (0)
#define BMM(A, B) do { UNR for (int i = 0; i < 9; i++) UNR for (int j = 0; j < 2; j++) acc[i][j] = MFMA16(A[i], B[j], acc[i][j]); } while (0)
#define BSG() do { UNR for (int i = 0; i < 13; i++) { SGB(0x8, 1); SGB(0x100, 1); } SGB(0x8, 18); } while (0)
    int slot = 0;
    for (int t = 0; t < 18; t++) {
        BAR(); LAS const char* base = (LAS const char*)lds + slot * B_STG;
        SB(); BRD(xa, xb, 0); BMM(ya, yb); BSG(); SB(); BRD(ya, yb, 1); BMM(xa, xb); BSG(); SB();
        WAITL0(); slot = (slot == B_NS - 1) ? 0 : slot + 1;
    }
    BMM(ya, yb);
    const __amdgpu_buffer_rsrc_t sp = mksrd(ws + WS_PX, 64 * 4 * 3 * 9216);
    gu32* fl = (gu32*)(ws + WS_FL) + (tile * 4 + wid) * 32;
    if (wid != kq) {
        const unsigned o = ((tile * 4 + wid) * 3 + (kq > wid ? kq - 1 : kq)) * 9216;
        UNR for (int i = 0; i < 9; i++)
            __builtin_amdgcn_raw_buffer_store_b128((u32x4){cvtpk(acc[i][0][0], acc[i][0][1]), cvtpk(acc[i][0][2], acc[i][0][3]), cvtpk(acc[i][1][0], acc[i][1][1]), cvtpk(acc[i][1][2], acc[i][1][3])}, sp, lane * 16 + i * 1024, o, 16);
        waitv<0>();
        if (lane == 0) __hip_atomic_fetch_add(fl, 1u, RLX_AGENT);
        return;
    }
    u32x4 hp[2][4]; u32x2 hs[2]; float bs[2];
    UNR for (int j = 0; j < 2; j++) {
        const int s16 = st * 8 + wid * 2 + j; const char* hf = ws + WS_HF + (size_t)s16 * 18432 + dT * 4608;
        UNR for (int i = 0; i < 4; i++) hp[j][i] = *(const u32x4*)(hf + i * 1024 + lane * 16);
        hs[j] = *(const u32x2*)(hf + 4096 + lane * 8); bs[j] = blo(b2[s16 * 16 + s]);
    }
    for (unsigned spins = 0; rfl(__hip_atomic_load(fl, RLX_AGENT)) < 3u && spins < SPIN_CAP; spins++) __builtin_amdgcn_s_sleep(1);
    __builtin_amdgcn_fence(__ATOMIC_ACQUIRE, "wavefront");
    UNR for (int c = 0; c < 3; c++) UNR for (int i = 0; i < 9; i++) {
        const u32x4 v = __builtin_amdgcn_raw_buffer_load_b128(sp, lane * 16 + i * 1024, ((tile * 4 + wid) * 3 + c) * 9216, 16);
        acc[i][0][0] += blo(v[0]); acc[i][0][1] += bhi(v[0]); acc[i][0][2] += blo(v[1]); acc[i][0][3] += bhi(v[1]);
        acc[i][1][0] += blo(v[2]); acc[i][1][1] += bhi(v[2]); acc[i][1][2] += blo(v[3]); acc[i][1][3] += bhi(v[3]);
    }
    UNR for (int j = 0; j < 2; j++) {
        bf16_t* orow = out + (size_t)(st * 128 + wid * 32 + j * 16 + s) * D + dT * 144;
        UNR for (int i = 0; i < 4; i++) {
            const f32x4 a = acc[2 * i][j], b = acc[2 * i + 1][j]; const u32x4 hh = hp[j][i];
            *(u32x4*)(orow + 32 * i + 8 * g) = (u32x4){cvtpk(a[0] + bs[j] + blo(hh[0]), a[1] + bs[j] + bhi(hh[0])), cvtpk(a[2] + bs[j] + blo(hh[1]), a[3] + bs[j] + bhi(hh[1])),
                                                       cvtpk(b[0] + bs[j] + blo(hh[2]), b[1] + bs[j] + bhi(hh[2])), cvtpk(b[2] + bs[j] + blo(hh[3]), b[3] + bs[j] + bhi(hh[3]))};
        }
        const f32x4 a = acc[8][j];
        *(u32x2*)(orow + 128 + 4 * g) = (u32x2){cvtpk(a[0] + bs[j] + blo(hs[j][0]), a[1] + bs[j] + bhi(hs[j][0])), cvtpk(a[2] + bs[j] + blo(hs[j][1]), a[3] + bs[j] + bhi(hs[j][1]))};
    }
#undef BRD
#undef BMM
#undef BSG
}

extern "C" void kernel_launch(void* const* d_in, const int* in_sizes, int n_in, void* d_out, int out_size, void* d_ws, size_t ws_size, hipStream_t stream) {
    const bf16_t* x = (const bf16_t*)d_in[0]; const bf16_t* wqkv = (const bf16_t*)d_in[1]; const bf16_t* wo = (const bf16_t*)d_in[2];
    const bf16_t* w1 = (const bf16_t*)d_in[4]; const bf16_t* b1 = (const bf16_t*)d_in[5]; const bf16_t* w2 = (const bf16_t*)d_in[6]; const bf16_t* b2 = (const bf16_t*)d_in[7];
    char* ws = (char*)d_ws;
    k_qkv<<<256, 768, 0, stream>>>(x, wqkv, ws);
    k_attn<<<256, 576, 0, stream>>>(ws);
    k_wo<<<192, 512, 0, stream>>>(x, wo, ws);
    k_up<<<216, 512, 0, stream>>>(w1, b1, ws);
    k_down<<<256, 512, 0, stream>>>(w2, b2, ws, (bf16_t*)d_out);
}
```

```cpp
#include <hip/hip_runtime.h>
#include <stdint.h>

typedef unsigned short bf16_t;
typedef short bf16x8 __attribute__((ext_vector_type(8)));
typedef short s16x4 __attribute__((ext_vector_type(4)));
typedef float f32x2 __attribute__((ext_vector_type(2)));
typedef float f32x4 __attribute__((ext_vector_type(4)));
typedef float f32x16 __attribute__((ext_vector_type(16)));
typedef unsigned u32x2 __attribute__((ext_vector_type(2)));
typedef unsigned u32x4 __attribute__((ext_vector_type(4)));
typedef __bf16 bfx2 __attribute__((ext_vector_type(2)));
typedef __attribute__((address_space(1))) unsigned long long gu64;
typedef __attribute__((address_space(1))) unsigned gu32;
#define LAS __attribute__((address_space(3)))
#define DI __device__ __forceinline__
#define RLX_AGENT __ATOMIC_RELAXED, __HIP_MEMORY_SCOPE_AGENT

constexpr int S = 2048, D = 576, F = 4608;
constexpr int CHUNK = 18944, BLOB = 8 * CHUNK;
constexpr size_t WS_QB = 0;
constexpr size_t WS_QR = WS_QB + 32 * 64 * 1024;
constexpr size_t WS_KV = WS_QR + 32 * 2048 * 4;
constexpr size_t WS_OF = WS_KV + 32 * (size_t)BLOB;
constexpr size_t WS_HF = WS_OF + (size_t)S * D * 2;
constexpr size_t WS_HH = WS_HF + (size_t)S * D * 2;
constexpr size_t WS_UF = WS_HH + (size_t)S * D * 2;
constexpr size_t WS_ST = WS_UF + (size_t)D * F * 2;
constexpr size_t WS_FL = WS_ST + (size_t)S * 6 * 8;
constexpr size_t WS_PX = WS_FL + 256 * 128;
constexpr float EPS = 1e-5f;
constexpr float QSCALE = 0.23570226039551584f * 1.4426950408889634f;
constexpr unsigned SPIN_CAP = 2000000u;

DI unsigned rfl(unsigned x) { return __builtin_amdgcn_readfirstlane(x); }
DI __amdgpu_buffer_rsrc_t mksrd(const void* p, unsigned bytes) { return __builtin_amdgcn_make_buffer_rsrc((void*)p, (short)0, (int)bytes, 0x00020000); }
DI void dma16(unsigned ldsaddr, unsigned voff, __amdgpu_buffer_rsrc_t srd, unsigned soff) {
    asm volatile("s_mov_b32 m0, %0\n\ts_nop 0\n\tbuffer_load_dwordx4 %1, %2, %3 offen lds" ::"s"(ldsaddr), "v"(voff), "s"(srd), "s"(soff) : "memory");
}
DI void touch(unsigned voff, __amdgpu_buffer_rsrc_t srd, unsigned soff) {
    unsigned t; asm volatile("buffer_load_dword %0, %1, %2, %3 offen\n\ts_waitcnt vmcnt(3)" : "=v"(t) : "v"(voff), "s"(srd), "s"(soff) : "memory");
}
DI s16x4 trrd(LAS const char* p) { return __builtin_amdgcn_ds_read_tr16_b64_v4i16((LAS s16x4*)p); }
DI bf16x8 rd128(LAS const char* p) { return *(LAS const bf16x8*)p; }
DI bf16x8 cat(s16x4 a, s16x4 b) { return __builtin_shufflevector(a, b, 0, 1, 2, 3, 4, 5, 6, 7); }
DI unsigned cvtpk(float lo, float hi) { unsigned r; asm("v_cvt_pk_bf16_f32 %0, %1, %2" : "=v"(r) : "v"(lo), "v"(hi)); return r; }
DI float blo(unsigned x) { return __uint_as_float(x << 16); }
DI float bhi(unsigned x) { return __uint_as_float(x & 0xffff0000u); }
DI float dot2(unsigned a, unsigned b, float c) { return __builtin_amdgcn_fdot2_f32_bf16(__builtin_bit_cast(bfx2, a), __builtin_bit_cast(bfx2, b), c, false); }
DI float swapadd32(float v) { auto r = __builtin_amdgcn_permlane32_swap(__float_as_uint(v), __float_as_uint(v), false, false); return __uint_as_float(r[0]) + __uint_as_float(r[1]); }
DI float swapmax32(float v) { auto r = __builtin_amdgcn_permlane32_swap(__float_as_uint(v), __float_as_uint(v), false, false); return __builtin_fmaxf(__uint_as_float(r[0]), __uint_as_float(r[1])); }
DI float swapadd16(float v) { auto r = __builtin_amdgcn_permlane16_swap(__float_as_uint(v), __float_as_uint(v), false, false); return __uint_as_float(r[0]) + __uint_as_float(r[1]); }
DI float gelu1(float u) {
    const float t = __builtin_fmaf(u * u, -2.f * 0.7978845608028654f * 0.044715f * 1.4426950408889634f, -2.f * 0.7978845608028654f * 1.4426950408889634f);
    return u * __builtin_amdgcn_rcpf(1.f + __builtin_amdgcn_exp2f(t * u));
}
#define MFMA16(a, b, c) __builtin_amdgcn_mfma_f32_16x16x32_bf16(a, b, c, 0, 0, 0)
#define MFMA32(a, b, c) __builtin_amdgcn_mfma_f32_32x32x16_bf16(a, b, c, 0, 0, 0)
#define MFMA32K8(a, b, c) __builtin_amdgcn_mfma_f32_32x32x8bf16_1k(a, b, c, 0, 0, 0)
#define BAR() asm volatile("s_barrier" ::: "memory")
#define WAITL0() asm volatile("s_waitcnt lgkmcnt(0)" ::: "memory")
#define SB() __builtin_amdgcn_sched_barrier(0)
#define SGB(m, n) __builtin_amdgcn_sched_group_barrier(m, n, 0)
#define UNR _Pragma("unroll")
template <int N> DI void waitv() { asm volatile("s_waitcnt vmcnt(%0)" ::"i"(N) : "memory"); }
#define Z8 ((bf16x8){0, 0, 0, 0, 0, 0, 0, 0})

#define PROFILE(k)
#define STAMP(i)
#define PROFARG
#define PROFPASS
template <int NT, int NS, int P, class Issue> DI void ring_loader(Issue issue) {
    issue(0, 0); issue(1, 1);
    waitv<P>(); BAR();
    UNR for (int t = 2; t < NS; t++) issue(t, t);
    int slot = 0;
    for (int t = 1; t <= NT - NS; t++) { waitv<P*(NS - 2)>(); BAR(); issue(t + NS - 1, slot); slot = (slot == NS - 1) ? 0 : slot + 1; }
    if constexpr (NS >= 5) { waitv<P * 3>(); BAR(); }
    if constexpr (NS >= 4) { waitv<P * 2>(); BAR(); }
    waitv<P>(); BAR();
    waitv<0>(); BAR();
}

constexpr int Q_STG = 32768, Q_NS = 4, Q_ST = Q_NS * Q_STG;
template <bool VT> DI void qkv_consumer(char* lds, char* ws, int mt, int nt, int wid, int lane PROFARG) {
    const int wn = wid & 1, wsn = wid >> 1, r = lane & 31, h = lane >> 5, g = lane >> 4, q = (lane >> 2) & 3, p = lane & 3;
    unsigned xoff[2][2], woff[2];
    UNR for (int b = 0; b < 2; b++) { const int row = wsn * 64 + b * 32 + r; UNR for (int e = 0; e < 2; e++) xoff[b][e] = row * 128 + (((2 * e + h) ^ ((row >> 1) & 3)) | (((row >> 1) & 4))) * 16; }
    UNR for (int a = 0; a < 2; a++) woff[a] = 16384 + (8 * h + q) * 256 + ((((wn * 2 + a) * 2 + (g & 1)) ^ (q << 1)) * 32) + p * 8;
    f32x16 acc[2][2];
    UNR for (int a = 0; a < 2; a++) UNR for (int b = 0; b < 2; b++) acc[a][b] = (f32x16){};
    bf16x8 xw[2], xx[2], yw[2] = {Z8, Z8}, yx[2] = {Z8, Z8};
#define QRD(W, X, ks) do { W[0] = cat(trrd(base + woff[0] + (ks) * 4096), trrd(base + woff[0] + (ks) * 4096 + 1024)); X[0] = rd128(base + (xoff[0][(ks) & 1] ^ (((ks) >> 1) * 64))); \
        X[1] = rd128(base + (xoff[1][(ks) & 1] ^ (((ks) >> 1) * 64))); W[1] = cat(trrd(base + woff[1] + (ks) * 4096), trrd(base + woff[1] + (ks) * 4096 + 1024)); } while (0)
#define QMM(W, X) do { UNR for (int a = 0; a < 2; a++) UNR for (int b = 0; b < 2; b++) acc[a][b] = VT ? MFMA32(X[b], W[a], acc[a][b]) : MFMA32(W[a], X[b], acc[a][b]); } while (0)
#define QSG() do { UNR for (int i = 0; i < 3; i++) { SGB(0x8, 1); SGB(0x100, 2); } SGB(0x8, 4); } while (0)
    int slot = 0;
    for (int t = 0; t < 9; t++) {
        BAR(); if (t == 0) STAMP(0); LAS const char* base = (LAS const char*)lds + slot * Q_STG;
        SB(); QRD(xw, xx, 0); QMM(yw, yx); QSG(); SB(); QRD(yw, yx, 1); QMM(xw, xx); QSG();
        SB(); QRD(xw, xx, 2); QMM(yw, yx); QSG(); SB(); QRD(yw, yx, 3); QMM(xw, xx); QSG(); SB();
        WAITL0(); slot = (slot == Q_NS - 1) ? 0 : slot + 1;
    }
    QMM(yw, yx); STAMP(1);
    BAR(); STAMP(2);
    LAS const float* mu = (LAS const float*)(lds + Q_ST); LAS const float* rs = mu + 128; LAS const float* cs = mu + 256;
    if constexpr (!VT) {
        float m_[2], r_[2];
        UNR for (int b = 0; b < 2; b++) { m_[b] = mu[wsn * 64 + b * 32 + r]; r_[b] = rs[wsn * 64 + b * 32 + r]; }
        UNR for (int a = 0; a < 2; a++) UNR for (int m = 0; m < 4; m++) {
            const int nl = wn * 64 + a * 32 + 8 * m + 4 * h, n = nt * 128 + nl; const f32x4 c4 = *(LAS const f32x4*)(cs + nl);
            const bool isk = n >= D; const int Qd = (n - (isk ? D : 0)) >> 2, ph = (Qd * 57) >> 9, qm = Qd - 9 * ph;
            const int head = 2 * ph + (qm > 4), pq = qm > 4 ? qm - 5 : qm;
            UNR for (int b = 0; b < 2; b++) {
                const int T = mt * 4 + wsn * 2 + b; const float rr = isk ? r_[b] : r_[b] * QSCALE;
                float v[4]; UNR for (int e = 0; e < 4; e++) v[e] = __builtin_fmaf(-m_[b], c4[e], acc[a][b][4 * m + e]) * rr;
                const u32x2 o = {cvtpk(v[0], v[1]), cvtpk(v[2], v[3])};
                if (qm != 4) {
                    const size_t tb = isk ? WS_KV + (size_t)head * BLOB + (T >> 3) * CHUNK + (T & 7) * 1024 : WS_QB + (size_t)(head * 64 + T) * 1024;
                    *(u32x2*)(ws + tb + (pq & 1) * 512 + (pq >> 1) * 256 + r * 8) = o;
                } else {
                    const size_t tb = isk ? WS_KV + (size_t)head * BLOB + (T >> 3) * CHUNK + 8192 + (T & 7) * 128 : WS_QR + (size_t)(head * 64 + T) * 128;
                    *(unsigned*)(ws + tb + r * 4) = o[0];
                    *(unsigned*)(ws + tb + (isk ? BLOB : 8192) + r * 4) = o[1];
                }
            }
        }
    } else {
        UNR for (int a = 0; a < 2; a++) {
            const int nl = wn * 64 + a * 32 + r, col = nt * 128 + nl - 2 * D; const float c1 = cs[nl];
            const int head = (col * 3641) >> 16, d = col - 18 * head;
            UNR for (int b = 0; b < 2; b++) {
                const int T = mt * 4 + wsn * 2 + b;
                char* tb = ws + WS_KV + (size_t)head * BLOB + (T >> 3) * CHUNK + 9216 + (T & 7) * 1216 + h * 304 + d * 16;
                UNR for (int s2 = 0; s2 < 2; s2++) {
                    u32x4 o;
                    UNR for (int e2 = 0; e2 < 2; e2++) {
                        const int m = 2 * s2 + e2, tl = wsn * 64 + b * 32 + 8 * m + 4 * h; const f32x4 m4 = *(LAS const f32x4*)(mu + tl), r4 = *(LAS const f32x4*)(rs + tl);
                        float v[4]; UNR for (int e = 0; e < 4; e++) v[e] = __builtin_fmaf(-m4[e], c1, acc[a][b][4 * m + e]) * r4[e];
                        o[2 * e2] = cvtpk(v[0], v[1]); o[2 * e2 + 1] = cvtpk(v[2], v[3]);
                    }
                    if (col < D) { *(u32x4*)(tb + s2 * 608) = o; if (d == 17) *(u32x4*)(tb + s2 * 608 + 16) = (u32x4){0x3f803f80u, 0x3f803f80u, 0x3f803f80u, 0x3f803f80u}; }
                }
            }
        }
    }
#undef QRD
#undef QMM
#undef QSG
}
__global__ __launch_bounds__(768, 3) void k_qkv(const bf16_t* __restrict__ x, const bf16_t* __restrict__ w, char* __restrict__ ws) {
    __shared__ __attribute__((aligned(16))) char lds[Q_ST + 1536]; PROFILE(0);
    const int tid = threadIdx.x, lane = tid & 63, wid = rfl(tid >> 6), bid = blockIdx.x;
    if (bid >= 224) {
        const int i = (bid - 224) * 768 + tid;
        if (i < S * 6 * 8 / 16) *(u32x4*)(ws + WS_ST + (size_t)i * 16) = (u32x4){~0u, ~0u, ~0u, ~0u};
        if (i < 256 * 128 / 16) *(u32x4*)(ws + WS_FL + (size_t)i * 16) = (u32x4){0u, 0u, 0u, 0u};
        return;
    }
    const int xg = bid & 7, idx = bid >> 3, mt = (xg >> 1) * 4 + (idx & 3), nt = (xg & 1) * 7 + (idx >> 2);
    const unsigned lds0 = (unsigned)(uintptr_t)lds;
    if (wid < 4) { if (nt >= 9) qkv_consumer<true>(lds, ws, mt, nt, wid, lane PROFPASS); else qkv_consumer<false>(lds, ws, mt, nt, wid, lane PROFPASS); }
    else if (wid < 8) {
        const int lw = wid - 4; const __amdgpu_buffer_rsrc_t sx = mksrd(x, S * D * 2), sw = mksrd(w, D * 3 * D * 2);
        unsigned vx[2];
        UNR for (int e = 0; e < 2; e++) vx[e] = (mt * 128 + 32 * lw + (lane >> 3)) * 1152 + ((lane & 7) ^ (4 * e + (lane >> 4))) * 16;
        const int rq = lane >> 4, c = lane & 15; const unsigned vw = (16 * lw + rq) * 3456 + nt * 256 + ((((c >> 1) ^ (rq << 1)) * 2) + (c & 1)) * 16;
        ring_loader<9, Q_NS, 8>([&](int t, int slot) {
            const unsigned lb = lds0 + slot * Q_STG;
            UNR for (int i = 0; i < 4; i++) dma16(lb + 16384 + (16 * lw + 4 * i) * 256, vw, sw, (t * 64 + 4 * i) * 3456);
            UNR for (int i = 0; i < 4; i++) dma16(lb + (32 * lw + 8 * i) * 128, vx[i & 1], sx, 8 * i * 1152 + t * 128);
        });
        BAR();
    } else {
        const int sw = wid - 8, r = lane & 31, h = lane >> 5, g = lane >> 4, q = (lane >> 2) & 3, p = lane & 3, row = sw * 32 + r;
        const unsigned woff = 16384 + (8 * h + q) * 256 + (((sw * 2 + (g & 1)) ^ (q << 1)) * 32) + p * 8;
        float s1 = 0.f, s2 = 0.f, c1 = 0.f; int slot = 0;
        for (int t = 0; t < 9; t++) {
            BAR(); LAS const char* base = (LAS const char*)lds + slot * Q_STG;
            UNR for (int ks = 0; ks < 4; ks++) {
                const u32x4 xv = *(LAS const u32x4*)(base + row * 128 + (((2 * ks + h) ^ ((row >> 1) & 7)) * 16));
                const u32x2 w0 = __builtin_bit_cast(u32x2, trrd(base + woff + ks * 4096)), w1 = __builtin_bit_cast(u32x2, trrd(base + woff + ks * 4096 + 1024));
                UNR for (int e = 0; e < 4; e++) { s1 = dot2(xv[e], 0x3f803f80u, s1); s2 = dot2(xv[e], xv[e], s2); }
                c1 = dot2(w0[0], 0x3f803f80u, c1); c1 = dot2(w0[1], 0x3f803f80u, c1); c1 = dot2(w1[0], 0x3f803f80u, c1); c1 = dot2(w1[1], 0x3f803f80u, c1);
            }
            WAITL0(); slot = (slot == Q_NS - 1) ? 0 : slot + 1;
        }
        s1 = swapadd32(s1); s2 = swapadd32(s2); c1 = swapadd32(c1);
        const float m = s1 * (1.f / D), var = __builtin_fmaxf(__builtin_fmaf(-m, m, s2 * (1.f / D)), 0.f);
        LAS float* st = (LAS float*)(lds + Q_ST);
        if (h == 0) { st[row] = m; st[128 + row] = __builtin_amdgcn_rsqf(var + EPS); st[256 + row] = c1; }
        WAITL0(); BAR();
    }
}

#define AT_IT(SC, SN, c0, c1, c2, c3, c4, c5, c6, c7, c8, c9, c10, c11, c12, c13, c14, c15, KO0, KO1, KR, V1, V0) \
    "s_waitcnt lgkmcnt(2)\n v_mfma_f32_32x32x16_bf16 " SN ", v[56:59], v[70:73], 0\n ds_read2st64_b64 v[56:59], v76 offset0:" KO0 " offset1:" KO1 "\n" \
    "v_exp_f32 " c0 "," c0 "\n v_exp_f32 " c1 "," c1 "\n v_exp_f32 " c2 "," c2 "\n v_exp_f32 " c3 "," c3 "\n v_cvt_pk_bf16_f32 v48," c0 "," c1 "\n v_cvt_pk_bf16_f32 v49," c2 "," c3 "\n" \
    "v_mfma_f32_32x32x8_bf16 " SN ", v[60:61], v[74:75], " SN "\n ds_read_b32 v60, v77 offset:" KR "\n" \
    "v_exp_f32 " c4 "," c4 "\n v_exp_f32 " c5 "," c5 "\n v_exp_f32 " c6 "," c6 "\n v_exp_f32 " c7 "," c7 "\n v_cvt_pk_bf16_f32 v50," c4 "," c5 "\n v_cvt_pk_bf16_f32 v51," c6 "," c7 "\n" \
    "s_waitcnt lgkmcnt(2)\n v_mfma_f32_32x32x16_bf16 v[32:47], v[66:69], v[52:55], v[32:47]\n" \
    "v_exp_f32 " c8 "," c8 "\n v_exp_f32 " c9 "," c9 "\n v_exp_f32 " c10 "," c10 "\n v_exp_f32 " c11 "," c11 "\n v_cvt_pk_bf16_f32 v52," c8 "," c9 "\n v_cvt_pk_bf16_f32 v53," c10 "," c11 "\n" \
    "v_mfma_f32_32x32x16_bf16 v[32:47], v[62:65], v[48:51], v[32:47]\n" \
    "s_mov_b64 exec, s[36:37]\n ds_read_b128 v[66:69], v78 offset:" V1 "\n ds_read_b128 v[62:65], v78 offset:" V0 "\n s_mov_b64 exec, -1\n" \
    "v_exp_f32 " c12 "," c12 "\n v_exp_f32 " c13 "," c13 "\n v_exp_f32 " c14 "," c14 "\n v_exp_f32 " c15 "," c15 "\n v_cvt_pk_bf16_f32 v54," c12 "," c13 "\n v_cvt_pk_bf16_f32 v55," c14 "," c15 "\n"
#define AT_A(KO0, KO1, KR, V1, V0) AT_IT("v[0:15]", "v[16:31]", "v0", "v1", "v2", "v3", "v4", "v5", "v6", "v7", "v8", "v9", "v10", "v11", "v12", "v13", "v14", "v15", KO0, KO1, KR, V1, V0)
#define AT_B(KO0, KO1, KR, V1, V0) AT_IT("v[16:31]", "v[0:15]", "v16", "v17", "v18", "v19", "v20", "v21", "v22", "v23", "v24", "v25", "v26", "v27", "v28", "v29", "v30", "v31", KO0, KO1, KR, V1, V0)
#define AT_SWAP 6
__global__ __launch_bounds__(576, 3) void k_attn(char* __restrict__ ws, const bf16_t* __restrict__ wo, const bf16_t* __restrict__ w1, const bf16_t* __restrict__ w2) {
    __shared__ __attribute__((aligned(16))) char lds[BLOB + 10240]; PROFILE(1);
    const int tid = threadIdx.x, lane = tid & 63, wid = rfl(tid >> 6), bid = blockIdx.x;
    const int head = (bid & 7) * 4 + ((bid >> 3) & 3), qp = bid >> 5;
    const unsigned lds0 = (unsigned)(uintptr_t)lds;
    const __amdgpu_buffer_rsrc_t skv = mksrd(ws + WS_KV + (size_t)head * BLOB, BLOB);
    const unsigned v16 = lane * 16;
    if (wid == 8) {
        for (int pc = 19; pc < 60; pc++) dma16(lds0 + pc * 1024, v16, skv, pc * 1024);
        BAR();
        for (int pc = 60; pc < 148; pc += 4) { waitv<40>(); UNR for (int i = 0; i < 4; i++) dma16(lds0 + (pc + i) * 1024, v16, skv, (pc + i) * 1024); }
        waitv<0>(); BAR();
        { const int xg = bid & 7, i = bid >> 3; const __amdgpu_buffer_rsrc_t s0 = mksrd(wo, D * D * 2), s1 = mksrd(w1, S * F * 2), s2 = mksrd(w2, S * F * 2);
          for (int k = 0; k < 9; k++) touch(((xg >> 1) * 1152 + 36 * i + (lane >> 4)) * (S * 2) + (xg & 1) * 2048 + (lane & 15) * 128, s2, k * 4 * (S * 2));
          for (int k = 0; k < 10; k++) touch((64 * i + lane) * (F * 2) + ((9 * xg) >> 1) * 256, s1, k * 128);
          for (int k = 0; k < 3; k++) touch((i * 162 + lane) * 128, s0, k * 8192);
          waitv<0>(); }
        return;
    }
    dma16(lds0 + wid * 1024, v16, skv, wid * 1024); dma16(lds0 + (wid + 8) * 1024, v16, skv, (wid + 8) * 1024);
    if (wid < 3) dma16(lds0 + (wid + 16) * 1024, v16, skv, (wid + 16) * 1024);
    const int r = lane & 31, h = lane >> 5, T = qp * 8 + wid;
    const char* qb = ws + WS_QB + (size_t)(head * 64 + T) * 1024 + h * 256 + r * 8;
    const u32x2 q0 = *(const u32x2*)qb, q1 = *(const u32x2*)(qb + 512);
    const unsigned qrest = *(const unsigned*)(ws + WS_QR + (size_t)(head * 64 + T) * 128 + r * 4);
    const bf16x8 qm = __builtin_bit_cast(bf16x8, (u32x4){q0[0], q0[1], q1[0], q1[1]});
    u32x2 qr = {h ? 0u : qrest, 0u};
    waitv<0>(); STAMP(0); BAR(); STAMP(1);
    const unsigned ka = h * 256 + r * 8, kra = 8192 + r * 4, va = 9216 + h * 304 + r * 16;
    {
        LAS const char* b = (LAS const char*)lds;
        const u32x2 k0 = *(LAS const u32x2*)(b + ka), k1 = *(LAS const u32x2*)(b + ka + 512); const u32x2 kr = {*(LAS const unsigned*)(b + kra), 0u};
        f32x16 s = MFMA32(__builtin_bit_cast(bf16x8, (u32x4){k0[0], k0[1], k1[0], k1[1]}), qm, (f32x16){});
        s = MFMA32K8(__builtin_bit_cast(s16x4, kr), __builtin_bit_cast(s16x4, qr), s);
        float m = s[0]; UNR for (int i = 1; i < 16; i++) m = __builtin_fmaxf(m, s[i]);
        m = swapmax32(m);
        qr[1] = h ? 0u : (cvtpk(-m, 0.f) & 0xffffu);
    }
    f32x16 O; STAMP(2);
    {
        unsigned a0 = lds0 + ka, a1 = lds0 + kra, a2 = lds0 + va;
        asm volatile(
            "s_mov_b32 s36, 0x7ffff\n s_mov_b32 s37, 0x7ffff\n s_mov_b32 s38, 0\n"
            "s_setprio 1\n s_cmp_ge_u32 %[wid], 4\n s_cbranch_scc1 1f\n s_setprio 2\n 1:\n"
            "ds_read2st64_b64 v[56:59], v76 offset0:0 offset1:1\n ds_read_b32 v60, v77\n"
            "v_mov_b32 v61, 0x3f80\n"
            "v_mov_b32 v32, 0\n v_mov_b32 v33, 0\n v_mov_b32 v34, 0\n v_mov_b32 v35, 0\n v_mov_b32 v36, 0\n v_mov_b32 v37, 0\n v_mov_b32 v38, 0\n v_mov_b32 v39, 0\n"
            "v_mov_b32 v40, 0\n v_mov_b32 v41, 0\n v_mov_b32 v42, 0\n v_mov_b32 v43, 0\n v_mov_b32 v44, 0\n v_mov_b32 v45, 0\n v_mov_b32 v46, 0\n v_mov_b32 v47, 0\n"
            "v_mov_b32 v52, 0\n v_mov_b32 v53, 0\n v_mov_b32 v54, 0\n v_mov_b32 v55, 0\n"
            "v_mov_b32 v62, 0\n v_mov_b32 v63, 0\n v_mov_b32 v64, 0\n v_mov_b32 v65, 0\n v_mov_b32 v66, 0\n v_mov_b32 v67, 0\n v_mov_b32 v68, 0\n v_mov_b32 v69, 0\n"
            "s_waitcnt lgkmcnt(0)\n"
            "v_mfma_f32_32x32x16_bf16 v[0:15], v[56:59], v[70:73], 0\n v_mfma_f32_32x32x8_bf16 v[0:15], v[60:61], v[74:75], v[0:15]\n"
            "ds_read2st64_b64 v[56:59], v76 offset0:2 offset1:3\n ds_read_b32 v60, v77 offset:128\n"
            "s_mov_b64 exec, s[36:37]\n ds_read_b128 v[66:69], v78 offset:608\n ds_read_b128 v[62:65], v78\n s_mov_b64 exec, -1\n"
            "s_nop 7\n s_nop 7\n"
            "2:\n"
            AT_A("4", "5", "256", "608", "1216") AT_B("6", "7", "384", "1824", "2432") AT_A("8", "9", "512", "3040", "3648") AT_B("10", "11", "640", "4256", "4864")
            AT_A("12", "13", "768", "5472", "6080") AT_B("14", "15", "896", "6688", "7296")
            "s_cmp_lg_u32 s38, 0\n s_cbranch_scc1 3f\n s_barrier\n 3:\n"
            AT_A("37", "38", "18944", "7904", "8512") AT_B("39", "40", "19072", "9120", "18944")
            "v_add_u32 v76, 0x4a00, v76\n v_add_u32 v77, 0x4a00, v77\n v_add_u32 v78, 0x4a00, v78\n"
            "s_add_u32 s38, s38, 1\n s_cmp_lg_u32 s38, %[swap]\n s_cbranch_scc1 4f\n s_cmp_ge_u32 %[wid], 4\n s_cbranch_scc1 4f\n s_setprio 0\n 4:\n"
            "s_cmp_lt_u32 s38, 8\n s_cbranch_scc1 2b\n"
            "s_waitcnt lgkmcnt(0)\n v_mfma_f32_32x32x16_bf16 v[32:47], v[66:69], v[52:55], v[32:47]\n s_setprio 0\n s_nop 7\n s_nop 7\n"
            : "={v[32:47]}"(O), "+{v76}"(a0), "+{v77}"(a1), "+{v78}"(a2)
            : "{v[70:73]}"(qm), "{v[74:75]}"(qr), [wid] "s"(wid), [swap] "i"(AT_SWAP)
            : "memory", "scc", "s36", "s37", "s38", "v0", "v1", "v2", "v3", "v4", "v5", "v6", "v7", "v8", "v9", "v10", "v11", "v12", "v13", "v14", "v15", "v16", "v17", "v18", "v19", "v20", "v21", "v22", "v23",
              "v24", "v25", "v26", "v27", "v28", "v29", "v30", "v31", "v48", "v49", "v50", "v51", "v52", "v53", "v54", "v55", "v56", "v57", "v58", "v59", "v60", "v61", "v62", "v63", "v64", "v65", "v66", "v67", "v68", "v69");
    }
    STAMP(3);
    float l = __shfl(O[10], r);
    const bool bad = !(l < 1.8e19f && l > 1e-18f);
    if (__builtin_expect(__any(bad), 0)) {
        LAS const char* b = (LAS const char*)lds; qr[1] = 0u; float m = -INFINITY;
        const unsigned vs = 9216 + h * 304 + (r < 19 ? r : r - 16) * 16;
        for (int pass = 0; pass < 2; pass++) {
            O = (f32x16){};
            for (int t = 0; t < 64; t++) {
                LAS const char* cb = b + (t >> 3) * CHUNK; const int i = t & 7;
                const u32x2 k0 = *(LAS const u32x2*)(cb + i * 1024 + ka), k1 = *(LAS const u32x2*)(cb + i * 1024 + ka + 512); const u32x2 kr = {*(LAS const unsigned*)(cb + i * 128 + kra), 0u};
                f32x16 s = MFMA32(__builtin_bit_cast(bf16x8, (u32x4){k0[0], k0[1], k1[0], k1[1]}), qm, (f32x16){});
                s = MFMA32K8(__builtin_bit_cast(s16x4, kr), __builtin_bit_cast(s16x4, qr), s);
                if (pass == 0) { UNR for (int e = 0; e < 16; e++) m = __builtin_fmaxf(m, s[e]); }
                else {
                    u32x4 p0, p1;
                    UNR for (int e = 0; e < 4; e++) { p0[e] = cvtpk(__builtin_amdgcn_exp2f(s[2 * e] - m), __builtin_amdgcn_exp2f(s[2 * e + 1] - m)); p1[e] = cvtpk(__builtin_amdgcn_exp2f(s[8 + 2 * e] - m), __builtin_amdgcn_exp2f(s[9 + 2 * e] - m)); }
                    asm volatile("s_nop 1" : "+v"(p0), "+v"(p1));
                    O = MFMA32(rd128(cb + i * 1216 + vs), __builtin_bit_cast(bf16x8, p0), O); O = MFMA32(rd128(cb + i * 1216 + 608 + vs), __builtin_bit_cast(bf16x8, p1), O);
                }
            }
            m = swapmax32(m);
        }
        l = __shfl(O[10], r);
    }
    const float il = 1.f / l; const int qrow = T * 32 + r;
    char* of = ws + WS_OF + (size_t)(qrow >> 4) * 18432;
    *(u32x4*)(of + (head >> 1) * 1024 + ((2 * (head & 1) + h) * 16 + (r & 15)) * 16) = (u32x4){cvtpk(O[0] * il, O[1] * il), cvtpk(O[2] * il, O[3] * il), cvtpk(O[4] * il, O[5] * il), cvtpk(O[6] * il, O[7] * il)};
    if (h == 0) *(unsigned*)(of + (16 + (head >> 4)) * 1024 + (((head & 15) >> 2) * 16 + (r & 15)) * 16 + (head & 3) * 4) = cvtpk(O[8] * il, O[9] * il);
}

constexpr int O_X = 9 * 12288, O_T = O_X + 12288;
__global__ __launch_bounds__(512, 2) void k_wo(const bf16_t* __restrict__ x, const bf16_t* __restrict__ wo, char* __restrict__ ws) {
    __shared__ __attribute__((aligned(16))) char lds[O_T + 4 * 3328]; PROFILE(2);
    const int tid = threadIdx.x, lane = tid & 63, wid = rfl(tid >> 6), bid = blockIdx.x;
    const int idx = bid >> 3, st = (bid & 7) * 4 + idx / 6, nt = idx % 6;
    const unsigned lds0 = (unsigned)(uintptr_t)lds;
    if (wid >= 4) {
        const int lw = wid - 4; const __amdgpu_buffer_rsrc_t sw = mksrd(wo, D * D * 2), sx = mksrd(x, S * D * 2);
        unsigned vm[3], vr[3], vx[3];
        UNR for (int i = 0; i < 3; i++) {
            const int c = i * 64 + lane, rl = c / 12, cc = c - 12 * rl, rho = 16 * lw + rl, g = (rho >> 2) & 3, j = (rho & 3) + 4 * ((rho >> 4) & 1), kb = rho >> 5;
            const int col = nt * 192 + ((((cc >> 1) ^ (g & 1)) * 2) + (cc & 1)) * 16;
            vm[i] = (36 * kb + 18 * (g >> 1) + 4 * (g & 1) + (j & 3) + 8 * (j >> 2)) * 1152 + col;
            vr[i] = (18 * (16 * kb + 4 * g + (j >> 1)) + 16 + (j & 1)) * 1152 + col;
            vx[i] = (st * 64 + rho) * 1152 + nt * 192 + cc * 16;
        }
        auto issue = [&](int t) {
            UNR for (int i = 0; i < 3; i++) {
                if (t < 8) dma16(lds0 + t * 12288 + (3 * lw + i) * 1024, vm[i], sw, t * 72 * 1152);
                else if (t == 8) dma16(lds0 + t * 12288 + (3 * lw + i) * 1024, vr[i], sw, 0);
                else dma16(lds0 + O_X + (3 * lw + i) * 1024, vx[i], sx, 0);
            } };
        issue(0); issue(1); waitv<3>(); BAR(); issue(2); issue(3); issue(4);
        UNR for (int t = 1; t <= 5; t++) { waitv<9>(); BAR(); issue(t + 4); }
        waitv<9>(); BAR(); waitv<6>(); BAR(); waitv<3>(); BAR(); waitv<0>(); BAR();
        return;
    }
    const int s = lane & 15, g = lane >> 4, q = (lane >> 2) & 3, p = lane & 3, s16 = st * 4 + wid;
    bf16x8 of[18];
    UNR for (int b = 0; b < 18; b++) of[b] = *(const bf16x8*)(ws + WS_OF + (size_t)s16 * 18432 + b * 1024 + lane * 16);
    const unsigned be = (4 * g + q) * 192 + (g & 1) * 32 + 8 * p, bo = (4 * g + q) * 192 - (g & 1) * 32 + 8 * p;
    f32x4 acc[6];
    UNR for (int t = 0; t < 6; t++) acc[t] = (f32x4){0.f, 0.f, 0.f, 0.f};
    UNR for (int t = 0; t < 9; t++) {
        BAR(); if (t == 0) STAMP(0); LAS const char* base = (LAS const char*)lds + t * 12288;
        UNR for (int kb = 0; kb < 2; kb++) UNR for (int n = 0; n < 6; n++) {
            LAS const char* a = base + kb * 6144 + ((n & 1) ? bo : be) + n * 32;
            acc[n] = MFMA16(cat(trrd(a), trrd(a + 3072)), of[2 * t + kb], acc[n]);
        }
    }
    STAMP(1); BAR(); STAMP(2);
    float hv[6][4], s1 = 0.f, s2 = 0.f;
    char* hf = ws + WS_HF + (size_t)s16 * 18432;
    UNR for (int n = 0; n < 6; n++) {
        const u32x2 xv = *(LAS const u32x2*)((LAS const char*)lds + O_X + (wid * 16 + s) * 192 + n * 32 + g * 8);
        const unsigned a0 = cvtpk(acc[n][0], acc[n][1]), a1 = cvtpk(acc[n][2], acc[n][3]);
        const u32x2 o = {cvtpk(blo(xv[0]) + blo(a0), bhi(xv[0]) + bhi(a0)), cvtpk(blo(xv[1]) + blo(a1), bhi(xv[1]) + bhi(a1))};
        hv[n][0] = blo(o[0]); hv[n][1] = bhi(o[0]); hv[n][2] = blo(o[1]); hv[n][3] = bhi(o[1]);
        UNR for (int e = 0; e < 4; e++) { s1 += hv[n][e]; s2 = __builtin_fmaf(hv[n][e], hv[n][e], s2); }
        const int t16 = nt * 6 + n, dT = t16 / 9, tl = t16 - 9 * dT;
        const unsigned off = tl < 8 ? (tl >> 1) * 1024 + ((2 * (tl & 1) + (g >> 1)) * 16 + s) * 16 + (g & 1) * 8 : 4096 + lane * 8;
        *(u32x2*)(hf + dT * 4608 + off) = o;
    }
    s1 = swapadd32(swapadd16(s1)); s2 = swapadd32(swapadd16(s2));
    gu64* gs = (gu64*)(ws + WS_ST) + (size_t)(s16 * 16 + s) * 6;
    { unsigned b2 = __float_as_uint(s2); if (b2 == ~0u) b2 = 0x7fc00000u;
      if (g == 0) __hip_atomic_store(gs + nt, ((unsigned long long)b2 << 32) | __float_as_uint(s1), RLX_AGENT); }
    STAMP(3);
    unsigned long long w0, w1 = 0;
    for (unsigned spins = 0;; spins++) {
        w0 = __hip_atomic_load(gs + g, RLX_AGENT); if (g < 2) w1 = __hip_atomic_load(gs + g + 4, RLX_AGENT);
        if (__all((unsigned)(w0 >> 32) != ~0u && (unsigned)(w1 >> 32) != ~0u) || spins > SPIN_CAP) break;
        __builtin_amdgcn_s_sleep(1);
    }
    STAMP(4);
    const float t1 = swapadd32(swapadd16(__uint_as_float((unsigned)w0) + __uint_as_float((unsigned)w1)));
    const float t2 = swapadd32(swapadd16(__uint_as_float((unsigned)(w0 >> 32)) + __uint_as_float((unsigned)(w1 >> 32))));
    const float m = t1 * (1.f / D), var = __builtin_fmaxf(__builtin_fmaf(-m, m, t2 * (1.f / D)), 0.f);
    const float r1 = __builtin_amdgcn_rsqf(var + EPS), rr = r1 * __builtin_amdgcn_rsqf(var * r1 * r1 + EPS);
    LAS char* tb = (LAS char*)lds + O_T + wid * 3328;
    UNR for (int n = 0; n < 6; n++) *(LAS u32x2*)(tb + s * 208 + n * 32 + g * 8) = (u32x2){cvtpk((hv[n][0] - m) * rr, (hv[n][1] - m) * rr), cvtpk((hv[n][2] - m) * rr, (hv[n][3] - m) * rr)};
    WAITL0();
    UNR for (int dt = 0; dt < 3; dt++) {
        LAS const char* a = tb + (4 * (g >> 1) + q) * 208 + dt * 64 + (g & 1) * 32 + p * 8;
        *(bf16x8*)(ws + WS_HH + (size_t)((nt * 128 + s16) * 3 + dt) * 1024 + lane * 16) = cat(trrd(a), trrd(a + 8 * 208));
    }
}

constexpr int A_STG = 28 * 1024, A_NS = 5;
__global__ __launch_bounds__(512, 2) void k_up(const bf16_t* __restrict__ w1, const bf16_t* __restrict__ b1, char* __restrict__ ws) {
    __shared__ __attribute__((aligned(16))) char lds[A_NS * A_STG]; PROFILE(3);
    const int tid = threadIdx.x, lane = tid & 63, wid = rfl(tid >> 6), bid = blockIdx.x;
    const int L = (bid & 7) * 27 + (bid >> 3), ft = L / 6, dT = L % 6;
    const unsigned lds0 = (unsigned)(uintptr_t)lds;
    if (wid >= 4) {
        const int lw = wid - 4, rq = lane >> 4, c = lane & 15; const __amdgpu_buffer_rsrc_t sw = mksrd(w1, S * F * 2), sh = mksrd(ws + WS_HH, S * D * 2);
        const unsigned vw = (16 * lw + rq) * (F * 2) + ft * 256 + ((((c >> 1) ^ (rq << 1)) * 2) + (c & 1)) * 16, vh = lane * 16;
        ring_loader<32, A_NS, 7>([&](int t, int slot) {
            const unsigned lb = lds0 + slot * A_STG;
            UNR for (int i = 0; i < 4; i++) dma16(lb + (16 * lw + 4 * i) * 256, vw, sw, (t * 64 + 4 * i) * (F * 2));
            UNR for (int j = 0; j < 3; j++) dma16(lb + 16384 + (lw * 3 + j) * 1024, vh, sh, (dT * 32 + t) * 12288 + (lw * 3 + j) * 1024);
        });
        BAR(); BAR();
        return;
    }
    const int wf = wid & 1, kh = wid >> 1, r = lane & 31, h = lane >> 5, g = lane >> 4, q = (lane >> 2) & 3, p = lane & 3;
    const int f0 = ft * 128 + (wf * 2 + kh) * 32;
    u32x2 bb[4];
    UNR for (int m = 0; m < 4; m++) bb[m] = *(const u32x2*)(b1 + f0 + 8 * m + 4 * h);
    unsigned aoff[2];
    UNR for (int i = 0; i < 2; i++) aoff[i] = kh * 8192 + (4 * h + q) * 256 + ((((wf * 2 + i) * 2 + (g & 1)) ^ (q << 1)) * 32) + p * 8;
    const unsigned boff = 16384 + kh * 6144 + lane * 16;
    f32x16 acc[2][3];
    UNR for (int i = 0; i < 2; i++) UNR for (int j = 0; j < 3; j++) acc[i][j] = (f32x16){};
    bf16x8 xa[2], xb[3], ya[2] = {Z8, Z8}, yb[3] = {Z8, Z8, Z8};
#define ARD(A, B, ks) do { B[0] = rd128(base + boff + (ks) * 3072); A[0] = cat(trrd(base + aoff[0] + (ks) * 4096), trrd(base + aoff[0] + (ks) * 4096 + 2048)); B[1] = rd128(base + boff + (ks) * 3072 + 1024); \
        B[2] = rd128(base + boff + (ks) * 3072 + 2048); A[1] = cat(trrd(base + aoff[1] + (ks) * 4096), trrd(base + aoff[1] + (ks) * 4096 + 2048)); } while (0)
#define AMM(A, B) do { UNR for (int i = 0; i < 2; i++) UNR for (int j = 0; j < 3; j++) acc[i][j] = MFMA32(A[i], B[j], acc[i][j]); } while (0)
#define ASG() do { UNR for (int i = 0; i < 3; i++) { SGB(0x8, 1); SGB(0x100, 3); } SGB(0x8, 6); } while (0)
    int slot = 0;
    for (int t = 0; t < 32; t++) {
        BAR(); if (t == 0) STAMP(0); LAS const char* base = (LAS const char*)lds + slot * A_STG;
        SB(); ARD(xa, xb, 0); AMM(ya, yb); ASG(); SB(); ARD(ya, yb, 1); AMM(xa, xb); ASG(); SB();
        WAITL0(); slot = (slot == A_NS - 1) ? 0 : slot + 1;
    }
    AMM(ya, yb); STAMP(1);
    BAR();
    LAS f32x4* ex = (LAS f32x4*)lds + wid * 12 * 64 + lane;
    UNR for (int j = 0; j < 3; j++) UNR for (int m = 0; m < 4; m++) {
        f32x4 v; UNR for (int e = 0; e < 4; e++) v[e] = kh ? acc[0][j][4 * m + e] : acc[1][j][4 * m + e];
        ex[(j * 4 + m) * 64] = v; }
    WAITL0(); BAR();
    STAMP(2);
    LAS const f32x4* ey = (LAS const f32x4*)lds + (wid ^ 2) * 12 * 64 + lane;
    UNR for (int j = 0; j < 3; j++) {
        const int d = dT * 96 + j * 32 + r, dT9 = d / 144, dl = d - 144 * dT9;
        const int tl = dl < 128 ? 2 * (dl >> 5) + ((dl >> 2) & 1) : 8, rho = dl < 128 ? 4 * ((dl >> 3) & 3) + (dl & 3) : dl - 128;
        char* ub = ws + WS_UF + (size_t)((dT9 * 144 + (f0 >> 5)) * 9 + tl) * 1024 + (h * 16 + rho) * 16;
        UNR for (int s2 = 0; s2 < 2; s2++) {
            u32x4 o;
            UNR for (int e2 = 0; e2 < 2; e2++) {
                const int m = 2 * s2 + e2; const f32x4 y = ey[(j * 4 + m) * 64]; float v[4];
                UNR for (int e = 0; e < 4; e++) v[e] = y[e] + (kh ? acc[1][j][4 * m + e] : acc[0][j][4 * m + e]);
                o[2 * e2] = cvtpk(gelu1(v[0] + blo(bb[m][0])), gelu1(v[1] + bhi(bb[m][0]))); o[2 * e2 + 1] = cvtpk(gelu1(v[2] + blo(bb[m][1])), gelu1(v[3] + bhi(bb[m][1])));
            }
            *(u32x4*)(ub + s2 * 512) = o;
        }
    }
#undef ARD
#undef AMM
#undef ASG
}

constexpr int B_STG = 34 * 1024, B_NS = 4;
__global__ __launch_bounds__(512, 2) void k_down(const bf16_t* __restrict__ w2, const bf16_t* __restrict__ b2, char* __restrict__ ws, bf16_t* __restrict__ out) {
    __shared__ __attribute__((aligned(16))) char lds[B_NS * B_STG]; PROFILE(4);
    const int tid = threadIdx.x, lane = tid & 63, wid = rfl(tid >> 6), bid = blockIdx.x;
    const int xg = bid & 7, idx = bid >> 3, kq = xg >> 1, st = (xg & 1) * 8 + (idx >> 2), dT = idx & 3, tile = st * 4 + dT;
    const unsigned lds0 = (unsigned)(uintptr_t)lds;
    if (wid >= 4) {
        const int lw = wid - 4, rq = lane >> 4, c = lane & 15; const __amdgpu_buffer_rsrc_t sw = mksrd(w2, S * F * 2), su = mksrd(ws + WS_UF, D * F * 2);
        unsigned vw[2];
        UNR for (int e = 0; e < 2; e++) vw[e] = (kq * 1152 + 16 * lw + rq) * (S * 2) + st * 256 + ((((c >> 1) ^ (4 * e + rq)) * 2) + (c & 1)) * 16;
        const unsigned vu = lane * 16;
        const int u0 = lw < 2 ? lw * 5 : 10 + (lw - 2) * 4, ul = lw < 2 ? 4 : 3;
        ring_loader<18, B_NS, 9>([&](int t, int slot) {
            const unsigned lb = lds0 + slot * B_STG;
            UNR for (int i = 0; i < 4; i++) dma16(lb + (16 * lw + 4 * i) * 256, vw[i & 1], sw, (t * 64 + 4 * i) * (S * 2));
            UNR for (int j = 0; j < 5; j++) { const int pc = u0 + (j < 4 ? j : ul); dma16(lb + 16384 + pc * 1024, vu, su, (dT * 144 + kq * 36 + 2 * t) * 9216 + pc * 1024); }
        });
        return;
    }
    const int s = lane & 15, g = lane >> 4, q = (lane >> 2) & 3, p = lane & 3, r7 = 4 * (g & 1) + q;
    unsigned boff[2];
    UNR for (int j = 0; j < 2; j++) boff[j] = (16 * (g >> 1) + r7) * 256 + (((wid * 2 + j) ^ r7) * 32) + p * 8;
    const unsigned aoff = 16384 + lane * 16;
    f32x4 acc[9][2];
    UNR for (int i = 0; i < 9; i++) UNR for (int j = 0; j < 2; j++) acc[i][j] = (f32x4){0.f, 0.f, 0.f, 0.f};
    bf16x8 xa[9], xb[2], ya[9], yb[2] = {Z8, Z8};
    UNR for (int i = 0; i < 9; i++) ya[i] = Z8;
#define BRD(A, B, ks) do { B[0] = cat(trrd(base + boff[0] + (ks) * 8192), trrd(base + boff[0] + (ks) * 8192 + 2048)); A[0] = rd128(base + aoff + (ks) * 9216); \
        B[1] = cat(trrd(base + boff[1] + (ks) * 8192), trrd(base + boff[1] + (ks) * 8192 + 2048)); UNR for (int i = 1; i < 9; i++) A[i] = rd128(base + aoff + (ks) * 9216 + i * 1024); } while (0)
#define BMM(A, B) do { UNR for (int i = 0; i < 9; i++) UNR for (int j = 0; j < 2; j++) acc[i][j] = MFMA16(A[i], B[j], acc[i][j]); } while (0)
#define BSG() do { UNR for (int i = 0; i < 13; i++) { SGB(0x8, 1); SGB(0x100, 1); } SGB(0x8, 18); } while (0)
    int slot = 0;
    for (int t = 0; t < 18; t++) {
        BAR(); if (t == 0) STAMP(0); LAS const char* base = (LAS const char*)lds + slot * B_STG;
        SB(); BRD(xa, xb, 0); BMM(ya, yb); BSG(); SB(); BRD(ya, yb, 1); BMM(xa, xb); BSG(); SB();
        WAITL0(); slot = (slot == B_NS - 1) ? 0 : slot + 1;
    }
    BMM(ya, yb); STAMP(1);
    const __amdgpu_buffer_rsrc_t sp = mksrd(ws + WS_PX, 64 * 4 * 3 * 9216);
    gu32* fl = (gu32*)(ws + WS_FL) + (tile * 4 + wid) * 32;
    if (wid != kq) {
        const unsigned o = ((tile * 4 + wid) * 3 + (kq > wid ? kq - 1 : kq)) * 9216;
        UNR for (int i = 0; i < 9; i++)
            __builtin_amdgcn_raw_buffer_store_b128((u32x4){cvtpk(acc[i][0][0], acc[i][0][1]), cvtpk(acc[i][0][2], acc[i][0][3]), cvtpk(acc[i][1][0], acc[i][1][1]), cvtpk(acc[i][1][2], acc[i][1][3])}, sp, lane * 16 + i * 1024, o, 16);
        STAMP(2); waitv<0>(); STAMP(3);
        if (lane == 0) __hip_atomic_fetch_add(fl, 1u, RLX_AGENT);
        return;
    }
    u32x4 hp[2][4]; u32x2 hs[2]; float bs[2];
    UNR for (int j = 0; j < 2; j++) {
        const int s16 = st * 8 + wid * 2 + j; const char* hf = ws + WS_HF + (size_t)s16 * 18432 + dT * 4608;
        UNR for (int i = 0; i < 4; i++) hp[j][i] = *(const u32x4*)(hf + i * 1024 + lane * 16);
        hs[j] = *(const u32x2*)(hf + 4096 + lane * 8); bs[j] = blo(b2[s16 * 16 + s]);
    }
    STAMP(2);
    for (unsigned spins = 0; rfl(__hip_atomic_load(fl, RLX_AGENT)) < 3u && spins < SPIN_CAP; spins++) __builtin_amdgcn_s_sleep(1);
    STAMP(3);
    __builtin_amdgcn_fence(__ATOMIC_ACQUIRE, "wavefront");
    UNR for (int c = 0; c < 3; c++) UNR for (int i = 0; i < 9; i++) {
        const u32x4 v = __builtin_amdgcn_raw_buffer_load_b128(sp, lane * 16 + i * 1024, ((tile * 4 + wid) * 3 + c) * 9216, 16);
        acc[i][0][0] += blo(v[0]); acc[i][0][1] += bhi(v[0]); acc[i][0][2] += blo(v[1]); acc[i][0][3] += bhi(v[1]);
        acc[i][1][0] += blo(v[2]); acc[i][1][1] += bhi(v[2]); acc[i][1][2] += blo(v[3]); acc[i][1][3] += bhi(v[3]);
    }
    STAMP(4);
    UNR for (int j = 0; j < 2; j++) {
        bf16_t* orow = out + (size_t)(st * 128 + wid * 32 + j * 16 + s) * D + dT * 144;
        UNR for (int i = 0; i < 4; i++) {
            const f32x4 a = acc[2 * i][j], b = acc[2 * i + 1][j]; const u32x4 hh = hp[j][i];
            *(u32x4*)(orow + 32 * i + 8 * g) = (u32x4){cvtpk(a[0] + bs[j] + blo(hh[0]), a[1] + bs[j] + bhi(hh[0])), cvtpk(a[2] + bs[j] + blo(hh[1]), a[3] + bs[j] + bhi(hh[1])),
                                                       cvtpk(b[0] + bs[j] + blo(hh[2]), b[1] + bs[j] + bhi(hh[2])), cvtpk(b[2] + bs[j] + blo(hh[3]), b[3] + bs[j] + bhi(hh[3]))};
        }
        const f32x4 a = acc[8][j];
        *(u32x2*)(orow + 128 + 4 * g) = (u32x2){cvtpk(a[0] + bs[j] + blo(hs[j][0]), a[1] + bs[j] + bhi(hs[j][0])), cvtpk(a[2] + bs[j] + blo(hs[j][1]), a[3] + bs[j] + bhi(hs[j][1]))};
    }
#undef BRD
#undef BMM
#undef BSG
}

extern "C" void kernel_launch(void* const* d_in, const int* in_sizes, int n_in, void* d_out, int out_size, void* d_ws, size_t ws_size, hipStream_t stream) {
    const bf16_t* x = (const bf16_t*)d_in[0]; const bf16_t* wqkv = (const bf16_t*)d_in[1]; const bf16_t* wo = (const bf16_t*)d_in[2];
    const bf16_t* w1 = (const bf16_t*)d_in[4]; const bf16_t* b1 = (const bf16_t*)d_in[5]; const bf16_t* w2 = (const bf16_t*)d_in[6]; const bf16_t* b2 = (const bf16_t*)d_in[7];
    char* ws = (char*)d_ws;
    k_qkv<<<256, 768, 0, stream>>>(x, wqkv, ws);
    k_attn<<<256, 576, 0, stream>>>(ws, wo, w1, w2);
    k_wo<<<192, 512, 0, stream>>>(x, wo, ws);
    k_up<<<216, 512, 0, stream>>>(w1, b1, ws);
    k_down<<<256, 512, 0, stream>>>(w2, b2, ws, (bf16_t*)d_out);
}
```

```cpp
#define PROF
#define PROBE 2
#include <hip/hip_runtime.h>
#include <stdint.h>

typedef unsigned short bf16_t;
typedef short bf16x8 __attribute__((ext_vector_type(8)));
typedef short s16x4 __attribute__((ext_vector_type(4)));
typedef float f32x2 __attribute__((ext_vector_type(2)));
typedef float f32x4 __attribute__((ext_vector_type(4)));
typedef float f32x16 __attribute__((ext_vector_type(16)));
typedef unsigned u32x2 __attribute__((ext_vector_type(2)));
typedef unsigned u32x4 __attribute__((ext_vector_type(4)));
typedef __bf16 bfx2 __attribute__((ext_vector_type(2)));
typedef __attribute__((address_space(1))) unsigned long long gu64;
typedef __attribute__((address_space(1))) unsigned gu32;
#define LAS __attribute__((address_space(3)))
#define DI __device__ __forceinline__
#define RLX_AGENT __ATOMIC_RELAXED, __HIP_MEMORY_SCOPE_AGENT

constexpr int S = 2048, D = 576, F = 4608;
constexpr int CHUNK = 18944, BLOB = 8 * CHUNK;
constexpr size_t WS_QB = 0;
constexpr size_t WS_QR = WS_QB + 32 * 64 * 1024;
constexpr size_t WS_KV = WS_QR + 32 * 2048 * 4;
constexpr size_t WS_OF = WS_KV + 32 * (size_t)BLOB;
constexpr size_t WS_HF = WS_OF + (size_t)S * D * 2;
constexpr size_t WS_HH = WS_HF + (size_t)S * D * 2;
constexpr size_t WS_UF = WS_HH + (size_t)S * D * 2;
constexpr size_t WS_ST = WS_UF + (size_t)D * F * 2;
constexpr size_t WS_FL = WS_ST + (size_t)S * 6 * 8;
constexpr size_t WS_PX = WS_FL + 256 * 128;
constexpr float EPS = 1e-5f;
constexpr float QSCALE = 0.23570226039551584f * 1.4426950408889634f;
constexpr unsigned SPIN_CAP = 2000000u;

DI unsigned rfl(unsigned x) { return __builtin_amdgcn_readfirstlane(x); }
DI __amdgpu_buffer_rsrc_t mksrd(const void* p, unsigned bytes) { return __builtin_amdgcn_make_buffer_rsrc((void*)p, (short)0, (int)bytes, 0x00020000); }
DI void dma16(unsigned ldsaddr, unsigned voff, __amdgpu_buffer_rsrc_t srd, unsigned soff) {
    asm volatile("s_mov_b32 m0, %0\n\ts_nop 0\n\tbuffer_load_dwordx4 %1, %2, %3 offen lds" ::"s"(ldsaddr), "v"(voff), "s"(srd), "s"(soff) : "memory");
}
DI void touch(unsigned voff, __amdgpu_buffer_rsrc_t srd, unsigned soff) {
    unsigned t; asm volatile("buffer_load_dword %0, %1, %2, %3 offen\n\ts_waitcnt vmcnt(3)" : "=v"(t) : "v"(voff), "s"(srd), "s"(soff) : "memory");
}
DI s16x4 trrd(LAS const char* p) { return __builtin_amdgcn_ds_read_tr16_b64_v4i16((LAS s16x4*)p); }
DI bf16x8 rd128(LAS const char* p) { return *(LAS const bf16x8*)p; }
DI bf16x8 cat(s16x4 a, s16x4 b) { return __builtin_shufflevector(a, b, 0, 1, 2, 3, 4, 5, 6, 7); }
DI unsigned cvtpk(float lo, float hi) { unsigned r; asm("v_cvt_pk_bf16_f32 %0, %1, %2" : "=v"(r) : "v"(lo), "v"(hi)); return r; }
DI float blo(unsigned x) { return __uint_as_float(x << 16); }
DI float bhi(unsigned x) { return __uint_as_float(x & 0xffff0000u); }
DI float dot2(unsigned a, unsigned b, float c) { return __builtin_amdgcn_fdot2_f32_bf16(__builtin_bit_cast(bfx2, a), __builtin_bit_cast(bfx2, b), c, false); }
DI float swapadd32(float v) { auto r = __builtin_amdgcn_permlane32_swap(__float_as_uint(v), __float_as_uint(v), false, false); return __uint_as_float(r[0]) + __uint_as_float(r[1]); }
DI float swapmax32(float v) { auto r = __builtin_amdgcn_permlane32_swap(__float_as_uint(v), __float_as_uint(v), false, false); return __builtin_fmaxf(__uint_as_float(r[0]), __uint_as_float(r[1])); }
DI float swapadd16(float v) { auto r = __builtin_amdgcn_permlane16_swap(__float_as_uint(v), __float_as_uint(v), false, false); return __uint_as_float(r[0]) + __uint_as_float(r[1]); }
DI float gelu1(float u) {
    const float t = __builtin_fmaf(u * u, -2.f * 0.7978845608028654f * 0.044715f * 1.4426950408889634f, -2.f * 0.7978845608028654f * 1.4426950408889634f);
    return u * __builtin_amdgcn_rcpf(1.f + __builtin_amdgcn_exp2f(t * u));
}
#define MFMA16(a, b, c) __builtin_amdgcn_mfma_f32_16x16x32_bf16(a, b, c, 0, 0, 0)
#define MFMA32(a, b, c) __builtin_amdgcn_mfma_f32_32x32x16_bf16(a, b, c, 0, 0, 0)
#define MFMA32K8(a, b, c) __builtin_amdgcn_mfma_f32_32x32x8bf16_1k(a, b, c, 0, 0, 0)
#define BAR() asm volatile("s_barrier" ::: "memory")
#define WAITL0() asm volatile("s_waitcnt lgkmcnt(0)" ::: "memory")
#define SB() __builtin_amdgcn_sched_barrier(0)
#define SGB(m, n) __builtin_amdgcn_sched_group_barrier(m, n, 0)
#define UNR _Pragma("unroll")
template <int N> DI void waitv() { asm volatile("s_waitcnt vmcnt(%0)" ::"i"(N) : "memory"); }
#define Z8 ((bf16x8){0, 0, 0, 0, 0, 0, 0, 0})

constexpr size_t WS_PROF = (size_t)64 << 20;
struct Prof { char* ws; int k; unsigned long long c0; unsigned st[8];
  DI Prof(char* w, int k_) : ws(w), k(k_) { c0 = __builtin_readcyclecounter(); UNR for (int i = 0; i < 8; i++) st[i] = 0; if ((threadIdx.x & 63) == 0) ((unsigned long long*)(ws + WS_PROF))[((k * 256 + blockIdx.x) * 16 + (threadIdx.x >> 6)) * 8] = __builtin_amdgcn_s_memrealtime(); }
  template <int I> DI void stamp() { SB(); unsigned long long t_; asm volatile("s_memtime %0\n\ts_waitcnt lgkmcnt(0)" : "=s"(t_) :: "memory"); st[I] = (unsigned)(t_ - c0); SB(); }
  DI ~Prof() { stamp<7>(); asm volatile("s_waitcnt vmcnt(0)" ::: "memory"); if ((threadIdx.x & 63) == 0) { unsigned long long* p = (unsigned long long*)(ws + WS_PROF) + ((k * 256 + blockIdx.x) * 16 + (threadIdx.x >> 6)) * 8; p[1] = __builtin_amdgcn_s_memrealtime(); p[2] = __builtin_readcyclecounter() - c0; p[3] = __builtin_amdgcn_s_getreg((3 << 11) | 20) & 15;
      UNR for (int i = 0; i < 4; i++) p[4 + i] = ((unsigned long long)st[2 * i + 1] << 32) | st[2 * i]; } } };
#define PROFILE(k) Prof prof_(ws, k)
#define STAMP(i) prof_.template stamp<i>()
#define PROFARG , Prof& prof_
#define PROFPASS , prof_
template <int NT, int NS, int P, class Issue> DI void ring_loader(Issue issue) {
    issue(0, 0); issue(1, 1);
    waitv<P>(); BAR();
    UNR for (int t = 2; t < NS; t++) issue(t, t);
    int slot = 0;
    for (int t = 1; t <= NT - NS; t++) { waitv<P*(NS - 2)>(); BAR(); issue(t + NS - 1, slot); slot = (slot == NS - 1) ? 0 : slot + 1; }
    if constexpr (NS >= 5) { waitv<P * 3>(); BAR(); }
    if constexpr (NS >= 4) { waitv<P * 2>(); BAR(); }
    waitv<P>(); BAR();
    waitv<0>(); BAR();
}

constexpr int Q_STG = 32768, Q_NS = 4, Q_ST = Q_NS * Q_STG;
template <bool VT> DI void qkv_consumer(char* lds, char* ws, int mt, int nt, int wid, int lane PROFARG) {
    const int wn = wid & 1, wsn = wid >> 1, r = lane & 31, h = lane >> 5, g = lane >> 4, q = (lane >> 2) & 3, p = lane & 3;
    unsigned xoff[2][2], woff[2];
    UNR for (int b = 0; b < 2; b++) { const int row = wsn * 64 + b * 32 + r; UNR for (int e = 0; e < 2; e++) xoff[b][e] = row * 128 + (((2 * e + h) ^ ((row >> 1) & 3)) | (((row >> 1) & 4))) * 16; }
    UNR for (int a = 0; a < 2; a++) woff[a] = 16384 + (8 * h + q) * 256 + ((((wn * 2 + a) * 2 + (g & 1)) ^ (q << 1)) * 32) + p * 8;
    f32x16 acc[2][2];
    UNR for (int a = 0; a < 2; a++) UNR for (int b = 0; b < 2; b++) acc[a][b] = (f32x16){};
    bf16x8 xw[2], xx[2], yw[2] = {Z8, Z8}, yx[2] = {Z8, Z8};
#define QRD(W, X, ks) do { W[0] = cat(trrd(base + woff[0] + (ks) * 4096), trrd(base + woff[0] + (ks) * 4096 + 1024)); X[0] = rd128(base + (xoff[0][(ks) & 1] ^ (((ks) >> 1) * 64))); \
        X[1] = rd128(base + (xoff[1][(ks) & 1] ^ (((ks) >> 1) * 64))); W[1] = cat(trrd(base + woff[1] + (ks) * 4096), trrd(base + woff[1] + (ks) * 4096 + 1024)); } while (0)
#define QMM(W, X) do { UNR for (int a = 0; a < 2; a++) UNR for (int b = 0; b < 2; b++) acc[a][b] = VT ? MFMA32(X[b], W[a], acc[a][b]) : MFMA32(W[a], X[b], acc[a][b]); } while (0)
#define QSG() do { UNR for (int i = 0; i < 3; i++) { SGB(0x8, 1); SGB(0x100, 2); } SGB(0x8, 4); } while (0)
    int slot = 0;
    for (int t = 0; t < 9; t++) {
        BAR(); if (t == 0) STAMP(0); LAS const char* base = (LAS const char*)lds + slot * Q_STG;
        SB(); QRD(xw, xx, 0); QMM(yw, yx); QSG(); SB(); QRD(yw, yx, 1); QMM(xw, xx); QSG();
        SB(); QRD(xw, xx, 2); QMM(yw, yx); QSG(); SB(); QRD(yw, yx, 3); QMM(xw, xx); QSG(); SB();
        WAITL0(); slot = (slot == Q_NS - 1) ? 0 : slot + 1;
    }
    QMM(yw, yx); STAMP(1);
    BAR(); STAMP(2);
    LAS const float* mu = (LAS const float*)(lds + Q_ST); LAS const float* rs = mu + 128; LAS const float* cs = mu + 256;
    if constexpr (!VT) {
        float m_[2], r_[2];
        UNR for (int b = 0; b < 2; b++) { m_[b] = mu[wsn * 64 + b * 32 + r]; r_[b] = rs[wsn * 64 + b * 32 + r]; }
        UNR for (int a = 0; a < 2; a++) UNR for (int m = 0; m < 4; m++) {
            const int nl = wn * 64 + a * 32 + 8 * m + 4 * h, n = nt * 128 + nl; const f32x4 c4 = *(LAS const f32x4*)(cs + nl);
            const bool isk = n >= D; const int Qd = (n - (isk ? D : 0)) >> 2, ph = (Qd * 57) >> 9, qm = Qd - 9 * ph;
            const int head = 2 * ph + (qm > 4), pq = qm > 4 ? qm - 5 : qm;
            UNR for (int b = 0; b < 2; b++) {
                const int T = mt * 4 + wsn * 2 + b; const float rr = isk ? r_[b] : r_[b] * QSCALE;
                float v[4]; UNR for (int e = 0; e < 4; e++) v[e] = __builtin_fmaf(-m_[b], c4[e], acc[a][b][4 * m + e]) * rr;
                const u32x2 o = {cvtpk(v[0], v[1]), cvtpk(v[2], v[3])};
                if (qm != 4) {
                    const size_t tb = isk ? WS_KV + (size_t)head * BLOB + (T >> 3) * CHUNK + (T & 7) * 1024 : WS_QB + (size_t)(head * 64 + T) * 1024;
                    *(u32x2*)(ws + tb + (pq & 1) * 512 + (pq >> 1) * 256 + r * 8) = o;
                } else {
                    const size_t tb = isk ? WS_KV + (size_t)head * BLOB + (T >> 3) * CHUNK + 8192 + (T & 7) * 128 : WS_QR + (size_t)(head * 64 + T) * 128;
                    *(unsigned*)(ws + tb + r * 4) = o[0];
                    *(unsigned*)(ws + tb + (isk ? BLOB : 8192) + r * 4) = o[1];
                }
            }
        }
    } else {
        UNR for (int a = 0; a < 2; a++) {
            const int nl = wn * 64 + a * 32 + r, col = nt * 128 + nl - 2 * D; const float c1 = cs[nl];
            const int head = (col * 3641) >> 16, d = col - 18 * head;
            UNR for (int b = 0; b < 2; b++) {
                const int T = mt * 4 + wsn * 2 + b;
                char* tb = ws + WS_KV + (size_t)head * BLOB + (T >> 3) * CHUNK + 9216 + (T & 7) * 1216 + h * 304 + d * 16;
                UNR for (int s2 = 0; s2 < 2; s2++) {
                    u32x4 o;
                    UNR for (int e2 = 0; e2 < 2; e2++) {
                        const int m = 2 * s2 + e2, tl = wsn * 64 + b * 32 + 8 * m + 4 * h; const f32x4 m4 = *(LAS const f32x4*)(mu + tl), r4 = *(LAS const f32x4*)(rs + tl);
                        float v[4]; UNR for (int e = 0; e < 4; e++) v[e] = __builtin_fmaf(-m4[e], c1, acc[a][b][4 * m + e]) * r4[e];
                        o[2 * e2] = cvtpk(v[0], v[1]); o[2 * e2 + 1] = cvtpk(v[2], v[3]);
                    }
                    if (col < D) { *(u32x4*)(tb + s2 * 608) = o; if (d == 17) *(u32x4*)(tb + s2 * 608 + 16) = (u32x4){0x3f803f80u, 0x3f803f80u, 0x3f803f80u, 0x3f803f80u}; }
                }
            }
        }
    }
#undef QRD
#undef QMM
#undef QSG
}
__global__ __launch_bounds__(768, 3) void k_qkv(const bf16_t* __restrict__ x, const bf16_t* __restrict__ w, char* __restrict__ ws) {
    __shared__ __attribute__((aligned(16))) char lds[Q_ST + 1536]; PROFILE(0);
    const int tid = threadIdx.x, lane = tid & 63, wid = rfl(tid >> 6), bid = blockIdx.x;
    if (bid >= 224) {
        const int i = (bid - 224) * 768 + tid;
        if (i < S * 6 * 8 / 16) *(u32x4*)(ws + WS_ST + (size_t)i * 16) = (u32x4){~0u, ~0u, ~0u, ~0u};
        if (i < 256 * 128 / 16) *(u32x4*)(ws + WS_FL + (size_t)i * 16) = (u32x4){0u, 0u, 0u, 0u};
        return;
    }
    const int xg = bid & 7, idx = bid >> 3, mt = (xg >> 1) * 4 + (idx & 3), nt = (xg & 1) * 7 + (idx >> 2);
    const unsigned lds0 = (unsigned)(uintptr_t)lds;
    if (wid < 4) { if (nt >= 9) qkv_consumer<true>(lds, ws, mt, nt, wid, lane PROFPASS); else qkv_consumer<false>(lds, ws, mt, nt, wid, lane PROFPASS); }
    else if (wid < 8) {
        const int lw = wid - 4; const __amdgpu_buffer_rsrc_t sx = mksrd(x, S * D * 2), sw = mksrd(w, D * 3 * D * 2);
        unsigned vx[2];
        UNR for (int e = 0; e < 2; e++) vx[e] = (mt * 128 + 32 * lw + (lane >> 3)) * 1152 + ((lane & 7) ^ (4 * e + (lane >> 4))) * 16;
        const int rq = lane >> 4, c = lane & 15; const unsigned vw = (16 * lw + rq) * 3456 + nt * 256 + ((((c >> 1) ^ (rq << 1)) * 2) + (c & 1)) * 16;
        ring_loader<9, Q_NS, 8>([&](int t, int slot) {
            const unsigned lb = lds0 + slot * Q_STG;
            UNR for (int i = 0; i < 4; i++) dma16(lb + 16384 + (16 * lw + 4 * i) * 256, vw, sw, (t * 64 + 4 * i) * 3456);
            UNR for (int i = 0; i < 4; i++) dma16(lb + (32 * lw + 8 * i) * 128, vx[i & 1], sx, 8 * i * 1152 + t * 128);
        });
        BAR();
    } else {
        const int sw = wid - 8, r = lane & 31, h = lane >> 5, g = lane >> 4, q = (lane >> 2) & 3, p = lane & 3, row = sw * 32 + r;
        const unsigned woff = 16384 + (8 * h + q) * 256 + (((sw * 2 + (g & 1)) ^ (q << 1)) * 32) + p * 8;
        float s1 = 0.f, s2 = 0.f, c1 = 0.f; int slot = 0;
        for (int t = 0; t < 9; t++) {
            BAR(); LAS const char* base = (LAS const char*)lds + slot * Q_STG;
            UNR for (int ks = 0; ks < 4; ks++) {
                const u32x4 xv = *(LAS const u32x4*)(base + row * 128 + (((2 * ks + h) ^ ((row >> 1) & 7)) * 16));
                const u32x2 w0 = __builtin_bit_cast(u32x2, trrd(base + woff + ks * 4096)), w1 = __builtin_bit_cast(u32x2, trrd(base + woff + ks * 4096 + 1024));
                UNR for (int e = 0; e < 4; e++) { s1 = dot2(xv[e], 0x3f803f80u, s1); s2 = dot2(xv[e], xv[e], s2); }
                c1 = dot2(w0[0], 0x3f803f80u, c1); c1 = dot2(w0[1], 0x3f803f80u, c1); c1 = dot2(w1[0], 0x3f803f80u, c1); c1 = dot2(w1[1], 0x3f803f80u, c1);
            }
            WAITL0(); slot = (slot == Q_NS - 1) ? 0 : slot + 1;
        }
        s1 = swapadd32(s1); s2 = swapadd32(s2); c1 = swapadd32(c1);
        const float m = s1 * (1.f / D), var = __builtin_fmaxf(__builtin_fmaf(-m, m, s2 * (1.f / D)), 0.f);
        LAS float* st = (LAS float*)(lds + Q_ST);
        if (h == 0) { st[row] = m; st[128 + row] = __builtin_amdgcn_rsqf(var + EPS); st[256 + row] = c1; }
        WAITL0(); BAR();
    }
}

#define AT_IT(SC, SN, c0, c1, c2, c3, c4, c5, c6, c7, c8, c9, c10, c11, c12, c13, c14, c15, KO0, KO1, KR, V1, V0) \
    "s_waitcnt lgkmcnt(2)\n v_mfma_f32_32x32x16_bf16 " SN ", v[56:59], v[70:73], 0\n ds_read2st64_b64 v[56:59], v76 offset0:" KO0 " offset1:" KO1 "\n" \
    "v_exp_f32 " c0 "," c0 "\n v_exp_f32 " c1 "," c1 "\n v_exp_f32 " c2 "," c2 "\n v_exp_f32 " c3 "," c3 "\n v_cvt_pk_bf16_f32 v48," c0 "," c1 "\n v_cvt_pk_bf16_f32 v49," c2 "," c3 "\n" \
    "v_mfma_f32_32x32x8_bf16 " SN ", v[60:61], v[74:75], " SN "\n ds_read_b32 v60, v77 offset:" KR "\n" \
    "v_exp_f32 " c4 "," c4 "\n v_exp_f32 " c5 "," c5 "\n v_exp_f32 " c6 "," c6 "\n v_exp_f32 " c7 "," c7 "\n v_cvt_pk_bf16_f32 v50," c4 "," c5 "\n v_cvt_pk_bf16_f32 v51," c6 "," c7 "\n" \
    "s_waitcnt lgkmcnt(2)\n v_mfma_f32_32x32x16_bf16 v[32:47], v[66:69], v[52:55], v[32:47]\n" \
    "v_exp_f32 " c8 "," c8 "\n v_exp_f32 " c9 "," c9 "\n v_exp_f32 " c10 "," c10 "\n v_exp_f32 " c11 "," c11 "\n v_cvt_pk_bf16_f32 v52," c8 "," c9 "\n v_cvt_pk_bf16_f32 v53," c10 "," c11 "\n" \
    "v_mfma_f32_32x32x16_bf16 v[32:47], v[62:65], v[48:51], v[32:47]\n" \
    "s_mov_b64 exec, s[36:37]\n ds_read_b128 v[66:69], v78 offset:" V1 "\n ds_read_b128 v[62:65], v78 offset:" V0 "\n s_mov_b64 exec, -1\n" \
    "v_exp_f32 " c12 "," c12 "\n v_exp_f32 " c13 "," c13 "\n v_exp_f32 " c14 "," c14 "\n v_exp_f32 " c15 "," c15 "\n v_cvt_pk_bf16_f32 v54," c12 "," c13 "\n v_cvt_pk_bf16_f32 v55," c14 "," c15 "\n"
#define AT_A(KO0, KO1, KR, V1, V0) AT_IT("v[0:15]", "v[16:31]", "v0", "v1", "v2", "v3", "v4", "v5", "v6", "v7", "v8", "v9", "v10", "v11", "v12", "v13", "v14", "v15", KO0, KO1, KR, V1, V0)
#define AT_B(KO0, KO1, KR, V1, V0) AT_IT("v[16:31]", "v[0:15]", "v16", "v17", "v18", "v19", "v20", "v21", "v22", "v23", "v24", "v25", "v26", "v27", "v28", "v29", "v30", "v31", KO0, KO1, KR, V1, V0)
#define AT_SWAP 6
__global__ __launch_bounds__(576, 3) void k_attn(char* __restrict__ ws, const bf16_t* __restrict__ wo, const bf16_t* __restrict__ w1, const bf16_t* __restrict__ w2) {
    __shared__ __attribute__((aligned(16))) char lds[BLOB + 10240]; PROFILE(1);
    const int tid = threadIdx.x, lane = tid & 63, wid = rfl(tid >> 6), bid = blockIdx.x;
    const int head = (bid & 7) * 4 + ((bid >> 3) & 3), qp = bid >> 5;
    const unsigned lds0 = (unsigned)(uintptr_t)lds;
    const __amdgpu_buffer_rsrc_t skv = mksrd(ws + WS_KV + (size_t)head * BLOB, BLOB);
    const unsigned v16 = lane * 16;
    if (wid == 8) {
        for (int pc = 19; pc < 60; pc++) dma16(lds0 + pc * 1024, v16, skv, pc * 1024);
        BAR();
        for (int pc = 60; pc < 148; pc += 4) { waitv<40>(); UNR for (int i = 0; i < 4; i++) dma16(lds0 + (pc + i) * 1024, v16, skv, (pc + i) * 1024); }
        waitv<0>(); BAR();
        { const int xg = bid & 7, i = bid >> 3; const __amdgpu_buffer_rsrc_t s0 = mksrd(wo, D * D * 2), s1 = mksrd(w1, S * F * 2), s2 = mksrd(w2, S * F * 2);
          for (int k = 0; k < 9; k++) touch(((xg >> 1) * 1152 + 36 * i + (lane >> 4)) * (S * 2) + (xg & 1) * 2048 + (lane & 15) * 128, s2, k * 4 * (S * 2));
          for (int k = 0; k < 10; k++) touch((64 * i + lane) * (F * 2) + ((9 * xg) >> 1) * 256, s1, k * 128);
          for (int k = 0; k < 3; k++) touch((i * 162 + lane) * 128, s0, k * 8192);
          waitv<0>(); }
        return;
    }
    dma16(lds0 + wid * 1024, v16, skv, wid * 1024); dma16(lds0 + (wid + 8) * 1024, v16, skv, (wid + 8) * 1024);
    if (wid < 3) dma16(lds0 + (wid + 16) * 1024, v16, skv, (wid + 16) * 1024);
    const int r = lane & 31, h = lane >> 5, T = qp * 8 + wid;
    const char* qb = ws + WS_QB + (size_t)(head * 64 + T) * 1024 + h * 256 + r * 8;
    const u32x2 q0 = *(const u32x2*)qb, q1 = *(const u32x2*)(qb + 512);
    const unsigned qrest = *(const unsigned*)(ws + WS_QR + (size_t)(head * 64 + T) * 128 + r * 4);
    const bf16x8 qm = __builtin_bit_cast(bf16x8, (u32x4){q0[0], q0[1], q1[0], q1[1]});
    u32x2 qr = {h ? 0u : qrest, 0u};
    waitv<0>(); STAMP(0); BAR(); STAMP(1);
    const unsigned ka = h * 256 + r * 8, kra = 8192 + r * 4, va = 9216 + h * 304 + r * 16;
    {
        LAS const char* b = (LAS const char*)lds;
        const u32x2 k0 = *(LAS const u32x2*)(b + ka), k1 = *(LAS const u32x2*)(b + ka + 512); const u32x2 kr = {*(LAS const unsigned*)(b + kra), 0u};
        f32x16 s = MFMA32(__builtin_bit_cast(bf16x8, (u32x4){k0[0], k0[1], k1[0], k1[1]}), qm, (f32x16){});
        s = MFMA32K8(__builtin_bit_cast(s16x4, kr), __builtin_bit_cast(s16x4, qr), s);
        float m = s[0]; UNR for (int i = 1; i < 16; i++) m = __builtin_fmaxf(m, s[i]);
        m = swapmax32(m);
        qr[1] = h ? 0u : (cvtpk(-m, 0.f) & 0xffffu);
    }
    f32x16 O; STAMP(2);
    {
        unsigned a0 = lds0 + ka, a1 = lds0 + kra, a2 = lds0 + va;
        asm volatile(
            "s_mov_b32 s36, 0x7ffff\n s_mov_b32 s37, 0x7ffff\n s_mov_b32 s38, 0\n"
            "s_setprio 1\n s_cmp_ge_u32 %[wid], 4\n s_cbranch_scc1 1f\n s_setprio 2\n 1:\n"
            "ds_read2st64_b64 v[56:59], v76 offset0:0 offset1:1\n ds_read_b32 v60, v77\n"
            "v_mov_b32 v61, 0x3f80\n"
            "v_mov_b32 v32, 0\n v_mov_b32 v33, 0\n v_mov_b32 v34, 0\n v_mov_b32 v35, 0\n v_mov_b32 v36, 0\n v_mov_b32 v37, 0\n v_mov_b32 v38, 0\n v_mov_b32 v39, 0\n"
            "v_mov_b32 v40, 0\n v_mov_b32 v41, 0\n v_mov_b32 v42, 0\n v_mov_b32 v43, 0\n v_mov_b32 v44, 0\n v_mov_b32 v45, 0\n v_mov_b32 v46, 0\n v_mov_b32 v47, 0\n"
            "v_mov_b32 v52, 0\n v_mov_b32 v53, 0\n v_mov_b32 v54, 0\n v_mov_b32 v55, 0\n"
            "v_mov_b32 v62, 0\n v_mov_b32 v63, 0\n v_mov_b32 v64, 0\n v_mov_b32 v65, 0\n v_mov_b32 v66, 0\n v_mov_b32 v67, 0\n v_mov_b32 v68, 0\n v_mov_b32 v69, 0\n"
            "s_waitcnt lgkmcnt(0)\n"
            "v_mfma_f32_32x32x16_bf16 v[0:15], v[56:59], v[70:73], 0\n v_mfma_f32_32x32x8_bf16 v[0:15], v[60:61], v[74:75], v[0:15]\n"
            "ds_read2st64_b64 v[56:59], v76 offset0:2 offset1:3\n ds_read_b32 v60, v77 offset:128\n"
            "s_mov_b64 exec, s[36:37]\n ds_read_b128 v[66:69], v78 offset:608\n ds_read_b128 v[62:65], v78\n s_mov_b64 exec, -1\n"
            "s_nop 7\n s_nop 7\n"
            "2:\n"
            AT_A("4", "5", "256", "608", "1216") AT_B("6", "7", "384", "1824", "2432") AT_A("8", "9", "512", "3040", "3648") AT_B("10", "11", "640", "4256", "4864")
            AT_A("12", "13", "768", "5472", "6080") AT_B("14", "15", "896", "6688", "7296")
            "s_cmp_lg_u32 s38, 0\n s_cbranch_scc1 3f\n s_barrier\n 3:\n"
            AT_A("37", "38", "18944", "7904", "8512") AT_B("39", "40", "19072", "9120", "18944")
            "v_add_u32 v76, 0x4a00, v76\n v_add_u32 v77, 0x4a00, v77\n v_add_u32 v78, 0x4a00, v78\n"
            "s_add_u32 s38, s38, 1\n s_cmp_lg_u32 s38, %[swap]\n s_cbranch_scc1 4f\n s_cmp_ge_u32 %[wid], 4\n s_cbranch_scc1 4f\n s_setprio 0\n 4:\n"
            "s_cmp_lt_u32 s38, 8\n s_cbranch_scc1 2b\n"
            "s_waitcnt lgkmcnt(0)\n v_mfma_f32_32x32x16_bf16 v[32:47], v[66:69], v[52:55], v[32:47]\n s_setprio 0\n s_nop 7\n s_nop 7\n"
            : "={v[32:47]}"(O), "+{v76}"(a0), "+{v77}"(a1), "+{v78}"(a2)
            : "{v[70:73]}"(qm), "{v[74:75]}"(qr), [wid] "s"(wid), [swap] "i"(AT_SWAP)
            : "memory", "scc", "s36", "s37", "s38", "v0", "v1", "v2", "v3", "v4", "v5", "v6", "v7", "v8", "v9", "v10", "v11", "v12", "v13", "v14", "v15", "v16", "v17", "v18", "v19", "v20", "v21", "v22", "v23",
              "v24", "v25", "v26", "v27", "v28", "v29", "v30", "v31", "v48", "v49", "v50", "v51", "v52", "v53", "v54", "v55", "v56", "v57", "v58", "v59", "v60", "v61", "v62", "v63", "v64", "v65", "v66", "v67", "v68", "v69");
    }
    STAMP(3);
    float l = __shfl(O[10], r);
    const bool bad = !(l < 1.8e19f && l > 1e-18f);
    if (__builtin_expect(__any(bad), 0)) {
        LAS const char* b = (LAS const char*)lds; qr[1] = 0u; float m = -INFINITY;
        const unsigned vs = 9216 + h * 304 + (r < 19 ? r : r - 16) * 16;
        for (int pass = 0; pass < 2; pass++) {
            O = (f32x16){};
            for (int t = 0; t < 64; t++) {
                LAS const char* cb = b + (t >> 3) * CHUNK; const int i = t & 7;
                const u32x2 k0 = *(LAS const u32x2*)(cb + i * 1024 + ka), k1 = *(LAS const u32x2*)(cb + i * 1024 + ka + 512); const u32x2 kr = {*(LAS const unsigned*)(cb + i * 128 + kra), 0u};
                f32x16 s = MFMA32(__builtin_bit_cast(bf16x8, (u32x4){k0[0], k0[1], k1[0], k1[1]}), qm, (f32x16){});
                s = MFMA32K8(__builtin_bit_cast(s16x4, kr), __builtin_bit_cast(s16x4, qr), s);
                if (pass == 0) { UNR for (int e = 0; e < 16; e++) m = __builtin_fmaxf(m, s[e]); }
                else {
                    u32x4 p0, p1;
                    UNR for (int e = 0; e < 4; e++) { p0[e] = cvtpk(__builtin_amdgcn_exp2f(s[2 * e] - m), __builtin_amdgcn_exp2f(s[2 * e + 1] - m)); p1[e] = cvtpk(__builtin_amdgcn_exp2f(s[8 + 2 * e] - m), __builtin_amdgcn_exp2f(s[9 + 2 * e] - m)); }
                    asm volatile("s_nop 1" : "+v"(p0), "+v"(p1));
                    O = MFMA32(rd128(cb + i * 1216 + vs), __builtin_bit_cast(bf16x8, p0), O); O = MFMA32(rd128(cb + i * 1216 + 608 + vs), __builtin_bit_cast(bf16x8, p1), O);
                }
            }
            m = swapmax32(m);
        }
        l = __shfl(O[10], r);
    }
    const float il = 1.f / l; const int qrow = T * 32 + r;
    char* of = ws + WS_OF + (size_t)(qrow >> 4) * 18432;
    *(u32x4*)(of + (head >> 1) * 1024 + ((2 * (head & 1) + h) * 16 + (r & 15)) * 16) = (u32x4){cvtpk(O[0] * il, O[1] * il), cvtpk(O[2] * il, O[3] * il), cvtpk(O[4] * il, O[5] * il), cvtpk(O[6] * il, O[7] * il)};
    if (h == 0) *(unsigned*)(of + (16 + (head >> 4)) * 1024 + (((head & 15) >> 2) * 16 + (r & 15)) * 16 + (head & 3) * 4) = cvtpk(O[8] * il, O[9] * il);
}

constexpr int O_X = 9 * 12288, O_T = O_X + 12288;
__global__ __launch_bounds__(512, 2) void k_wo(const bf16_t* __restrict__ x, const bf16_t* __restrict__ wo, char* __restrict__ ws) {
    __shared__ __attribute__((aligned(16))) char lds[O_T + 4 * 3328]; PROFILE(2);
    const int tid = threadIdx.x, lane = tid & 63, wid = rfl(tid >> 6), bid = blockIdx.x;
    const int idx = bid >> 3, st = (bid & 7) * 4 + idx / 6, nt = idx % 6;
    const unsigned lds0 = (unsigned)(uintptr_t)lds;
    if (wid >= 4) {
        const int lw = wid - 4; const __amdgpu_buffer_rsrc_t sw = mksrd(wo, D * D * 2), sx = mksrd(x, S * D * 2);
        unsigned vm[3], vr[3], vx[3];
        UNR for (int i = 0; i < 3; i++) {
            const int c = i * 64 + lane, rl = c / 12, cc = c - 12 * rl, rho = 16 * lw + rl, g = (rho >> 2) & 3, j = (rho & 3) + 4 * ((rho >> 4) & 1), kb = rho >> 5;
            const int col = nt * 192 + ((((cc >> 1) ^ (g & 1)) * 2) + (cc & 1)) * 16;
            vm[i] = (36 * kb + 18 * (g >> 1) + 4 * (g & 1) + (j & 3) + 8 * (j >> 2)) * 1152 + col;
            vr[i] = (18 * (16 * kb + 4 * g + (j >> 1)) + 16 + (j & 1)) * 1152 + col;
            vx[i] = (st * 64 + rho) * 1152 + nt * 192 + cc * 16;
        }
        auto issue = [&](int t) {
            UNR for (int i = 0; i < 3; i++) {
                if (t < 8) dma16(lds0 + t * 12288 + (3 * lw + i) * 1024, vm[i], sw, t * 72 * 1152);
                else if (t == 8) dma16(lds0 + t * 12288 + (3 * lw + i) * 1024, vr[i], sw, 0);
                else dma16(lds0 + O_X + (3 * lw + i) * 1024, vx[i], sx, 0);
            } };
        issue(0); issue(1); waitv<3>(); BAR(); issue(2); issue(3); issue(4);
        UNR for (int t = 1; t <= 5; t++) { waitv<9>(); BAR(); issue(t + 4); }
        waitv<9>(); BAR(); waitv<6>(); BAR(); waitv<3>(); BAR(); waitv<0>(); BAR();
        return;
    }
    const int s = lane & 15, g = lane >> 4, q = (lane >> 2) & 3, p = lane & 3, s16 = st * 4 + wid;
    bf16x8 of[18];
    UNR for (int b = 0; b < 18; b++) of[b] = *(const bf16x8*)(ws + WS_OF + (size_t)s16 * 18432 + b * 1024 + lane * 16);
    const unsigned be = (4 * g + q) * 192 + (g & 1) * 32 + 8 * p, bo = (4 * g + q) * 192 - (g & 1) * 32 + 8 * p;
    f32x4 acc[6];
    UNR for (int t = 0; t < 6; t++) acc[t] = (f32x4){0.f, 0.f, 0.f, 0.f};
    UNR for (int t = 0; t < 9; t++) {
        BAR(); if (t == 0) STAMP(0); LAS const char* base = (LAS const char*)lds + t * 12288;
        UNR for (int kb = 0; kb < 2; kb++) UNR for (int n = 0; n < 6; n++) {
            LAS const char* a = base + kb * 6144 + ((n & 1) ? bo : be) + n * 32;
            acc[n] = MFMA16(cat(trrd(a), trrd(a + 3072)), of[2 * t + kb], acc[n]);
        }
    }
    STAMP(1); BAR(); STAMP(2);
    float hv[6][4], s1 = 0.f, s2 = 0.f;
    char* hf = ws + WS_HF + (size_t)s16 * 18432;
    UNR for (int n = 0; n < 6; n++) {
        const u32x2 xv = *(LAS const u32x2*)((LAS const char*)lds + O_X + (wid * 16 + s) * 192 + n * 32 + g * 8);
        const unsigned a0 = cvtpk(acc[n][0], acc[n][1]), a1 = cvtpk(acc[n][2], acc[n][3]);
        const u32x2 o = {cvtpk(blo(xv[0]) + blo(a0), bhi(xv[0]) + bhi(a0)), cvtpk(blo(xv[1]) + blo(a1), bhi(xv[1]) + bhi(a1))};
        hv[n][0] = blo(o[0]); hv[n][1] = bhi(o[0]); hv[n][2] = blo(o[1]); hv[n][3] = bhi(o[1]);
        UNR for (int e = 0; e < 4; e++) { s1 += hv[n][e]; s2 = __builtin_fmaf(hv[n][e], hv[n][e], s2); }
        const int t16 = nt * 6 + n, dT = t16 / 9, tl = t16 - 9 * dT;
        const unsigned off = tl < 8 ? (tl >> 1) * 1024 + ((2 * (tl & 1) + (g >> 1)) * 16 + s) * 16 + (g & 1) * 8 : 4096 + lane * 8;
        *(u32x2*)(hf + dT * 4608 + off) = o;
    }
    s1 = swapadd32(swapadd16(s1)); s2 = swapadd32(swapadd16(s2));
    gu64* gs = (gu64*)(ws + WS_ST) + (size_t)(s16 * 16 + s) * 6;
    { unsigned b2 = __float_as_uint(s2); if (b2 == ~0u) b2 = 0x7fc00000u;
      if (g == 0) __hip_atomic_store(gs + nt, ((unsigned long long)b2 << 32) | __float_as_uint(s1), RLX_AGENT); }
    STAMP(3);
    unsigned long long w0, w1 = 0;
    for (unsigned spins = 0;; spins++) {
        w0 = __hip_atomic_load(gs + g, RLX_AGENT); if (g < 2) w1 = __hip_atomic_load(gs + g + 4, RLX_AGENT);
        if (__all((unsigned)(w0 >> 32) != ~0u && (unsigned)(w1 >> 32) != ~0u) || spins > SPIN_CAP) break;
        __builtin_amdgcn_s_sleep(1);
    }
    STAMP(4);
    const float t1 = swapadd32(swapadd16(__uint_as_float((unsigned)w0) + __uint_as_float((unsigned)w1)));
    const float t2 = swapadd32(swapadd16(__uint_as_float((unsigned)(w0 >> 32)) + __uint_as_float((unsigned)(w1 >> 32))));
    const float m = t1 * (1.f / D), var = __builtin_fmaxf(__builtin_fmaf(-m, m, t2 * (1.f / D)), 0.f);
    const float r1 = __builtin_amdgcn_rsqf(var + EPS), rr = r1 * __builtin_amdgcn_rsqf(var * r1 * r1 + EPS);
    LAS char* tb = (LAS char*)lds + O_T + wid * 3328;
    UNR for (int n = 0; n < 6; n++) *(LAS u32x2*)(tb + s * 208 + n * 32 + g * 8) = (u32x2){cvtpk((hv[n][0] - m) * rr, (hv[n][1] - m) * rr), cvtpk((hv[n][2] - m) * rr, (hv[n][3] - m) * rr)};
    WAITL0();
    UNR for (int dt = 0; dt < 3; dt++) {
        LAS const char* a = tb + (4 * (g >> 1) + q) * 208 + dt * 64 + (g & 1) * 32 + p * 8;
        *(bf16x8*)(ws + WS_HH + (size_t)((nt * 128 + s16) * 3 + dt) * 1024 + lane * 16) = cat(trrd(a), trrd(a + 8 * 208));
    }
}

constexpr int A_STG = 28 * 1024, A_NS = 5;
__global__ __launch_bounds__(512, 2) void k_up(const bf16_t* __restrict__ w1, const bf16_t* __restrict__ b1, char* __restrict__ ws) {
    __shared__ __attribute__((aligned(16))) char lds[A_NS * A_STG]; PROFILE(3);
    const int tid = threadIdx.x, lane = tid & 63, wid = rfl(tid >> 6), bid = blockIdx.x;
    const int L = (bid & 7) * 27 + (bid >> 3), ft = L / 6, dT = L % 6;
    const unsigned lds0 = (unsigned)(uintptr_t)lds;
    if (wid >= 4) {
        const int lw = wid - 4, rq = lane >> 4, c = lane & 15; const __amdgpu_buffer_rsrc_t sw = mksrd(w1, S * F * 2), sh = mksrd(ws + WS_HH, S * D * 2);
        const unsigned vw = (16 * lw + rq) * (F * 2) + ft * 256 + ((((c >> 1) ^ (rq << 1)) * 2) + (c & 1)) * 16, vh = lane * 16;
        ring_loader<32, A_NS, 7>([&](int t, int slot) {
            const unsigned lb = lds0 + slot * A_STG;
            UNR for (int i = 0; i < 4; i++) dma16(lb + (16 * lw + 4 * i) * 256, vw, sw, (t * 64 + 4 * i) * (F * 2));
            UNR for (int j = 0; j < 3; j++) dma16(lb + 16384 + (lw * 3 + j) * 1024, vh, sh, (dT * 32 + t) * 12288 + (lw * 3 + j) * 1024);
        });
        BAR(); BAR();
        return;
    }
    const int wf = wid & 1, kh = wid >> 1, r = lane & 31, h = lane >> 5, g = lane >> 4, q = (lane >> 2) & 3, p = lane & 3;
    const int f0 = ft * 128 + (wf * 2 + kh) * 32;
    u32x2 bb[4];
    UNR for (int m = 0; m < 4; m++) bb[m] = *(const u32x2*)(b1 + f0 + 8 * m + 4 * h);
    unsigned aoff[2];
    UNR for (int i = 0; i < 2; i++) aoff[i] = kh * 8192 + (4 * h + q) * 256 + ((((wf * 2 + i) * 2 + (g & 1)) ^ (q << 1)) * 32) + p * 8;
    const unsigned boff = 16384 + kh * 6144 + lane * 16;
    f32x16 acc[2][3];
    UNR for (int i = 0; i < 2; i++) UNR for (int j = 0; j < 3; j++) acc[i][j] = (f32x16){};
    bf16x8 xa[2], xb[3], ya[2] = {Z8, Z8}, yb[3] = {Z8, Z8, Z8};
#define ARD(A, B, ks) do { B[0] = rd128(base + boff + (ks) * 3072); A[0] = cat(trrd(base + aoff[0] + (ks) * 4096), trrd(base + aoff[0] + (ks) * 4096 + 2048)); B[1] = rd128(base + boff + (ks) * 3072 + 1024); \
        B[2] = rd128(base + boff + (ks) * 3072 + 2048); A[1] = cat(trrd(base + aoff[1] + (ks) * 4096), trrd(base + aoff[1] + (ks) * 4096 + 2048)); } while (0)
#define AMM(A, B) do { UNR for (int i = 0; i < 2; i++) UNR for (int j = 0; j < 3; j++) acc[i][j] = MFMA32(A[i], B[j], acc[i][j]); } while (0)
#define ASG() do { UNR for (int i = 0; i < 3; i++) { SGB(0x8, 1); SGB(0x100, 3); } SGB(0x8, 6); } while (0)
    int slot = 0;
    for (int t = 0; t < 32; t++) {
        BAR(); if (t == 0) STAMP(0); LAS const char* base = (LAS const char*)lds + slot * A_STG;
        SB(); ARD(xa, xb, 0); AMM(ya, yb); ASG(); SB(); ARD(ya, yb, 1); AMM(xa, xb); ASG(); SB();
        WAITL0(); slot = (slot == A_NS - 1) ? 0 : slot + 1;
    }
    AMM(ya, yb); STAMP(1);
    BAR();
    LAS f32x4* ex = (LAS f32x4*)lds + wid * 12 * 64 + lane;
    UNR for (int j = 0; j < 3; j++) UNR for (int m = 0; m < 4; m++) {
        f32x4 v; UNR for (int e = 0; e < 4; e++) v[e] = kh ? acc[0][j][4 * m + e] : acc[1][j][4 * m + e];
        ex[(j * 4 + m) * 64] = v; }
    WAITL0(); BAR();
    STAMP(2);
    LAS const f32x4* ey = (LAS const f32x4*)lds + (wid ^ 2) * 12 * 64 + lane;
    UNR for (int j = 0; j < 3; j++) {
        const int d = dT * 96 + j * 32 + r, dT9 = d / 144, dl = d - 144 * dT9;
        const int tl = dl < 128 ? 2 * (dl >> 5) + ((dl >> 2) & 1) : 8, rho = dl < 128 ? 4 * ((dl >> 3) & 3) + (dl & 3) : dl - 128;
        char* ub = ws + WS_UF + (size_t)((dT9 * 144 + (f0 >> 5)) * 9 + tl) * 1024 + (h * 16 + rho) * 16;
        UNR for (int s2 = 0; s2 < 2; s2++) {
            u32x4 o;
            UNR for (int e2 = 0; e2 < 2; e2++) {
                const int m = 2 * s2 + e2; const f32x4 y = ey[(j * 4 + m) * 64]; float v[4];
                UNR for (int e = 0; e < 4; e++) v[e] = y[e] + (kh ? acc[1][j][4 * m + e] : acc[0][j][4 * m + e]);
                o[2 * e2] = cvtpk(gelu1(v[0] + blo(bb[m][0])), gelu1(v[1] + bhi(bb[m][0]))); o[2 * e2 + 1] = cvtpk(gelu1(v[2] + blo(bb[m][1])), gelu1(v[3] + bhi(bb[m][1])));
            }
            *(u32x4*)(ub + s2 * 512) = o;
        }
    }
#undef ARD
#undef AMM
#undef ASG
}

constexpr int B_STG = 34 * 1024, B_NS = 4;
__global__ __launch_bounds__(512, 2) void k_down(const bf16_t* __restrict__ w2, const bf16_t* __restrict__ b2, char* __restrict__ ws, bf16_t* __restrict__ out) {
    __shared__ __attribute__((aligned(16))) char lds[B_NS * B_STG]; PROFILE(4);
    const int tid = threadIdx.x, lane = tid & 63, wid = rfl(tid >> 6), bid = blockIdx.x;
    const int xg = bid & 7, idx = bid >> 3, kq = xg >> 1, st = (xg & 1) * 8 + (idx >> 2), dT = idx & 3, tile = st * 4 + dT;
    const unsigned lds0 = (unsigned)(uintptr_t)lds;
    if (wid >= 4) {
        const int lw = wid - 4, rq = lane >> 4, c = lane & 15; const __amdgpu_buffer_rsrc_t sw = mksrd(w2, S * F * 2), su = mksrd(ws + WS_UF, D * F * 2);
        unsigned vw[2];
        UNR for (int e = 0; e < 2; e++) vw[e] = (kq * 1152 + 16 * lw + rq) * (S * 2) + st * 256 + ((((c >> 1) ^ (4 * e + rq)) * 2) + (c & 1)) * 16;
        const unsigned vu = lane * 16;
        const int u0 = lw < 2 ? lw * 5 : 10 + (lw - 2) * 4, ul = lw < 2 ? 4 : 3;
        ring_loader<18, B_NS, 9>([&](int t, int slot) {
            const unsigned lb = lds0 + slot * B_STG;
            UNR for (int i = 0; i < 4; i++) dma16(lb + (16 * lw + 4 * i) * 256, vw[i & 1], sw, (t * 64 + 4 * i) * (S * 2));
            UNR for (int j = 0; j < 5; j++) { const int pc = u0 + (j < 4 ? j : ul); dma16(lb + 16384 + pc * 1024, vu, su, (dT * 144 + kq * 36 + 2 * t) * 9216 + pc * 1024); }
        });
        return;
    }
    const int s = lane & 15, g = lane >> 4, q = (lane >> 2) & 3, p = lane & 3, r7 = 4 * (g & 1) + q;
    unsigned boff[2];
    UNR for (int j = 0; j < 2; j++) boff[j] = (16 * (g >> 1) + r7) * 256 + (((wid * 2 + j) ^ r7) * 32) + p * 8;
    const unsigned aoff = 16384 + lane * 16;
    f32x4 acc[9][2];
    UNR for (int i = 0; i < 9; i++) UNR for (int j = 0; j < 2; j++) acc[i][j] = (f32x4){0.f, 0.f, 0.f, 0.f};
    bf16x8 xa[9], xb[2], ya[9], yb[2] = {Z8, Z8};
    UNR for (int i = 0; i < 9; i++) ya[i] = Z8;
#define BRD(A, B, ks) do { B[0] = cat(trrd(base + boff[0] + (ks) * 8192), trrd(base + boff[0] + (ks) * 8192 + 2048)); A[0] = rd128(base + aoff + (ks) * 9216); \
        B[1] = cat(trrd(base + boff[1] + (ks) * 8192), trrd(base + boff[1] + (ks) * 8192 + 2048)); UNR for (int i = 1; i < 9; i++) A[i] = rd128(base + aoff + (ks) * 9216 + i * 1024); } while (0)
#define BMM(A, B) do { UNR for (int i = 0; i < 9; i++) UNR for (int j = 0; j < 2; j++) acc[i][j] = MFMA16(A[i], B[j], acc[i][j]); } while (0)
#define BSG() do { UNR for (int i = 0; i < 13; i++) { SGB(0x8, 1); SGB(0x100, 1); } SGB(0x8, 18); } while (0)
    int slot = 0;
    for (int t = 0; t < 18; t++) {
        BAR(); if (t == 0) STAMP(0); LAS const char* base = (LAS const char*)lds + slot * B_STG;
        SB(); BRD(xa, xb, 0); BMM(ya, yb); BSG(); SB(); BRD(ya, yb, 1); BMM(xa, xb); BSG(); SB();
        WAITL0(); slot = (slot == B_NS - 1) ? 0 : slot + 1;
    }
    BMM(ya, yb); STAMP(1);
    const __amdgpu_buffer_rsrc_t sp = mksrd(ws + WS_PX, 64 * 4 * 3 * 9216);
    gu32* fl = (gu32*)(ws + WS_FL) + (tile * 4 + wid) * 32;
    if (wid != kq) {
        const unsigned o = ((tile * 4 + wid) * 3 + (kq > wid ? kq - 1 : kq)) * 9216;
        UNR for (int i = 0; i < 9; i++)
            __builtin_amdgcn_raw_buffer_store_b128((u32x4){cvtpk(acc[i][0][0], acc[i][0][1]), cvtpk(acc[i][0][2], acc[i][0][3]), cvtpk(acc[i][1][0], acc[i][1][1]), cvtpk(acc[i][1][2], acc[i][1][3])}, sp, lane * 16 + i * 1024, o, 16);
        STAMP(2); waitv<0>(); STAMP(3);
        if (lane == 0) __hip_atomic_fetch_add(fl, 1u, RLX_AGENT);
        return;
    }
    u32x4 hp[2][4]; u32x2 hs[2]; float bs[2];
    UNR for (int j = 0; j < 2; j++) {
        const int s16 = st * 8 + wid * 2 + j; const char* hf = ws + WS_HF + (size_t)s16 * 18432 + dT * 4608;
        UNR for (int i = 0; i < 4; i++) hp[j][i] = *(const u32x4*)(hf + i * 1024 + lane * 16);
        hs[j] = *(const u32x2*)(hf + 4096 + lane * 8); bs[j] = blo(b2[s16 * 16 + s]);
    }
    STAMP(2);
    for (unsigned spins = 0; rfl(__hip_atomic_load(fl, RLX_AGENT)) < 3u && spins < SPIN_CAP; spins++) __builtin_amdgcn_s_sleep(1);
    STAMP(3);
    __builtin_amdgcn_fence(__ATOMIC_ACQUIRE, "wavefront");
    UNR for (int c = 0; c < 3; c++) UNR for (int i = 0; i < 9; i++) {
        const u32x4 v = __builtin_amdgcn_raw_buffer_load_b128(sp, lane * 16 + i * 1024, ((tile * 4 + wid) * 3 + c) * 9216, 16);
        acc[i][0][0] += blo(v[0]); acc[i][0][1] += bhi(v[0]); acc[i][0][2] += blo(v[1]); acc[i][0][3] += bhi(v[1]);
        acc[i][1][0] += blo(v[2]); acc[i][1][1] += bhi(v[2]); acc[i][1][2] += blo(v[3]); acc[i][1][3] += bhi(v[3]);
    }
    STAMP(4);
    UNR for (int j = 0; j < 2; j++) {
        bf16_t* orow = out + (size_t)(st * 128 + wid * 32 + j * 16 + s) * D + dT * 144;
        UNR for (int i = 0; i < 4; i++) {
            const f32x4 a = acc[2 * i][j], b = acc[2 * i + 1][j]; const u32x4 hh = hp[j][i];
            *(u32x4*)(orow + 32 * i + 8 * g) = (u32x4){cvtpk(a[0] + bs[j] + blo(hh[0]), a[1] + bs[j] + bhi(hh[0])), cvtpk(a[2] + bs[j] + blo(hh[1]), a[3] + bs[j] + bhi(hh[1])),
                                                       cvtpk(b[0] + bs[j] + blo(hh[2]), b[1] + bs[j] + bhi(hh[2])), cvtpk(b[2] + bs[j] + blo(hh[3]), b[3] + bs[j] + bhi(hh[3]))};
        }
        const f32x4 a = acc[8][j];
        *(u32x2*)(orow + 128 + 4 * g) = (u32x2){cvtpk(a[0] + bs[j] + blo(hs[j][0]), a[1] + bs[j] + bhi(hs[j][0])), cvtpk(a[2] + bs[j] + blo(hs[j][1]), a[3] + bs[j] + bhi(hs[j][1]))};
    }
#undef BRD
#undef BMM
#undef BSG
}

__global__ void k_probe(char* ws) {
    __shared__ unsigned long long sa[256], sb[256];
    const int nb[5] = {256, 256, 192, 216, 256}, nw[5] = {12, 9, 8, 8, 8};
    const unsigned long long* pr = (const unsigned long long*)(ws + WS_PROF);
    auto span = [&](int k, unsigned long long& a, unsigned long long& b) {
        unsigned long long x = ~0ull, y = 0; const int t = threadIdx.x;
        if (t < nb[k]) for (int w = 0; w < nw[k]; w++) { const unsigned long long* p = pr + ((k * 256 + t) * 16 + w) * 8; x = p[0] < x ? p[0] : x; y = p[1] > y ? p[1] : y; }
        sa[t] = x; sb[t] = y; __syncthreads();
        for (int i = 0; i < 256; i++) { x = sa[i] < x ? sa[i] : x; y = sb[i] > y ? sb[i] : y; }
        __syncthreads(); a = x; b = y; };
    unsigned long long X = 0, a, b, c, d;
    const int Q = PROBE;
    if (Q < 5) { span(Q, a, b); X = b - a; }
    else if (Q < 9) { span(Q - 5, a, b); span(Q - 4, c, d); X = c - b; }
    else if (Q == 9) { span(0, a, b); span(4, c, d); X = (d - a) / 4; }
    else if (Q < 20) { X = pr[(((Q - 10) * 256 + 5) * 16) * 8 + 2] / 20; }
    else { const int k = (Q - 20) / 8, i = (Q - 20) % 8; X = (unsigned)(pr[((k * 256 + 5) * 16) * 8 + 4 + i / 2] >> (32 * (i & 1))) / 20; }
    if (X > 5000) X = 5000;
    const unsigned long long t0 = __builtin_amdgcn_s_memrealtime();
    while (__builtin_amdgcn_s_memrealtime() - t0 < 20 * X) __builtin_amdgcn_s_sleep(2);
}
extern "C" void kernel_launch(void* const* d_in, const int* in_sizes, int n_in, void* d_out, int out_size, void* d_ws, size_t ws_size, hipStream_t stream) {
    const bf16_t* x = (const bf16_t*)d_in[0]; const bf16_t* wqkv = (const bf16_t*)d_in[1]; const bf16_t* wo = (const bf16_t*)d_in[2];
    const bf16_t* w1 = (const bf16_t*)d_in[4]; const bf16_t* b1 = (const bf16_t*)d_in[5]; const bf16_t* w2 = (const bf16_t*)d_in[6]; const bf16_t* b2 = (const bf16_t*)d_in[7];
    char* ws = (char*)d_ws;
    k_qkv<<<256, 768, 0, stream>>>(x, wqkv, ws);
    k_attn<<<256, 576, 0, stream>>>(ws, wo, w1, w2);
    k_wo<<<192, 512, 0, stream>>>(x, wo, ws);
    k_up<<<216, 512, 0, stream>>>(w1, b1, ws);
    k_down<<<256, 512, 0, stream>>>(w2, b2, ws, (bf16_t*)d_out);
    k_probe<<<1, 256, 0, stream>>>(ws);
}
```

```cpp
#include <hip/hip_runtime.h>
#include <stdint.h>

typedef unsigned short bf16_t;
typedef short bf16x8 __attribute__((ext_vector_type(8)));
typedef short s16x4 __attribute__((ext_vector_type(4)));
typedef float f32x2 __attribute__((ext_vector_type(2)));
typedef float f32x4 __attribute__((ext_vector_type(4)));
typedef float f32x16 __attribute__((ext_vector_type(16)));
typedef unsigned u32x2 __attribute__((ext_vector_type(2)));
typedef unsigned u32x4 __attribute__((ext_vector_type(4)));
typedef __bf16 bfx2 __attribute__((ext_vector_type(2)));
typedef __attribute__((address_space(1))) unsigned long long gu64;
typedef __attribute__((address_space(1))) unsigned gu32;
#define LAS __attribute__((address_space(3)))
#define DI __device__ __forceinline__
#define RLX_AGENT __ATOMIC_RELAXED, __HIP_MEMORY_SCOPE_AGENT

constexpr int S = 2048, D = 576, F = 4608;
constexpr int CHUNK = 18944, BLOB = 8 * CHUNK;
constexpr size_t WS_QB = 0;
constexpr size_t WS_QR = WS_QB + 32 * 64 * 1024;
constexpr size_t WS_KV = WS_QR + 32 * 2048 * 4;
constexpr size_t WS_OF = WS_KV + 32 * (size_t)BLOB;
constexpr size_t WS_HF = WS_OF + (size_t)S * D * 2;
constexpr size_t WS_HH = WS_HF + (size_t)S * D * 2;
constexpr size_t WS_UF = WS_HH + (size_t)S * D * 2;
constexpr size_t WS_ST = WS_UF + (size_t)D * F * 2;
constexpr size_t WS_FL = WS_ST + (size_t)S * 6 * 8;
constexpr size_t WS_PX = WS_FL + 256 * 128;
constexpr float EPS = 1e-5f;
constexpr float QSCALE = 0.23570226039551584f * 1.4426950408889634f;
constexpr unsigned SPIN_CAP = 2000000u;

DI unsigned rfl(unsigned x) { return __builtin_amdgcn_readfirstlane(x); }
DI __amdgpu_buffer_rsrc_t mksrd(const void* p, unsigned bytes) { return __builtin_amdgcn_make_buffer_rsrc((void*)p, (short)0, (int)bytes, 0x00020000); }
DI void dma16(unsigned ldsaddr, unsigned voff, __amdgpu_buffer_rsrc_t srd, unsigned soff) {
    asm volatile("s_mov_b32 m0, %0\n\ts_nop 0\n\tbuffer_load_dwordx4 %1, %2, %3 offen lds" ::"s"(ldsaddr), "v"(voff), "s"(srd), "s"(soff) : "memory");
}
DI void touch(unsigned& sink, unsigned voff, __amdgpu_buffer_rsrc_t srd, unsigned soff) {
    asm volatile("buffer_load_dword %0, %1, %2, %3 offen\n\ts_waitcnt vmcnt(3)" : "+v"(sink) : "v"(voff), "s"(srd), "s"(soff) : "memory");
}
DI s16x4 trrd(LAS const char* p) { return __builtin_amdgcn_ds_read_tr16_b64_v4i16((LAS s16x4*)p); }
DI bf16x8 rd128(LAS const char* p) { return *(LAS const bf16x8*)p; }
DI bf16x8 cat(s16x4 a, s16x4 b) { return __builtin_shufflevector(a, b, 0, 1, 2, 3, 4, 5, 6, 7); }
DI unsigned cvtpk(float lo, float hi) { unsigned r; asm("v_cvt_pk_bf16_f32 %0, %1, %2" : "=v"(r) : "v"(lo), "v"(hi)); return r; }
DI float blo(unsigned x) { return __uint_as_float(x << 16); }
DI float bhi(unsigned x) { return __uint_as_float(x & 0xffff0000u); }
DI float dot2(unsigned a, unsigned b, float c) { return __builtin_amdgcn_fdot2_f32_bf16(__builtin_bit_cast(bfx2, a), __builtin_bit_cast(bfx2, b), c, false); }
DI float swapadd32(float v) { auto r = __builtin_amdgcn_permlane32_swap(__float_as_uint(v), __float_as_uint(v), false, false); return __uint_as_float(r[0]) + __uint_as_float(r[1]); }
DI float swapmax32(float v) { auto r = __builtin_amdgcn_permlane32_swap(__float_as_uint(v), __float_as_uint(v), false, false); return __builtin_fmaxf(__uint_as_float(r[0]), __uint_as_float(r[1])); }
DI float swapadd16(float v) { auto r = __builtin_amdgcn_permlane16_swap(__float_as_uint(v), __float_as_uint(v), false, false); return __uint_as_float(r[0]) + __uint_as_float(r[1]); }
DI float gelu1(float u) {
    const float t = __builtin_fmaf(u * u, -2.f * 0.7978845608028654f * 0.044715f * 1.4426950408889634f, -2.f * 0.7978845608028654f * 1.4426950408889634f);
    return u * __builtin_amdgcn_rcpf(1.f + __builtin_amdgcn_exp2f(t * u));
}
#define MFMA16(a, b, c) __builtin_amdgcn_mfma_f32_16x16x32_bf16(a, b, c, 0, 0, 0)
#define MFMA32(a, b, c) __builtin_amdgcn_mfma_f32_32x32x16_bf16(a, b, c, 0, 0, 0)
#define MFMA32K8(a, b, c) __builtin_amdgcn_mfma_f32_32x32x8bf16_1k(a, b, c, 0, 0, 0)
#define BAR() asm volatile("s_barrier" ::: "memory")
#define WAITL0() asm volatile("s_waitcnt lgkmcnt(0)" ::: "memory")
#define SB() __builtin_amdgcn_sched_barrier(0)
#define SGB(m, n) __builtin_amdgcn_sched_group_barrier(m, n, 0)
#define UNR _Pragma("unroll")
template <int N> DI void waitv() { asm volatile("s_waitcnt vmcnt(%0)" ::"i"(N) : "memory"); }
#define Z8 ((bf16x8){0, 0, 0, 0, 0, 0, 0, 0})

#define PROFILE(k)
#define STAMP(i)
#define PROFARG
#define PROFPASS
template <int NT, int NS, int P, class Issue> DI void ring_loader(Issue issue) {
    issue(0, 0); issue(1, 1);
    waitv<P>(); BAR();
    UNR for (int t = 2; t < NS; t++) issue(t, t);
    int slot = 0;
    for (int t = 1; t <= NT - NS; t++) { waitv<P*(NS - 2)>(); BAR(); issue(t + NS - 1, slot); slot = (slot == NS - 1) ? 0 : slot + 1; }
    if constexpr (NS >= 5) { waitv<P * 3>(); BAR(); }
    if constexpr (NS >= 4) { waitv<P * 2>(); BAR(); }
    waitv<P>(); BAR();
    waitv<0>(); BAR();
}

constexpr int Q_STG = 32768, Q_NS = 4, Q_ST = Q_NS * Q_STG;
template <bool VT> DI void qkv_consumer(char* lds, char* ws, int mt, int nt, int wid, int lane PROFARG) {
    const int wn = wid & 1, wsn = wid >> 1, r = lane & 31, h = lane >> 5, g = lane >> 4, q = (lane >> 2) & 3, p = lane & 3;
    unsigned xoff[2][2], woff[2];
    UNR for (int b = 0; b < 2; b++) { const int row = wsn * 64 + b * 32 + r; UNR for (int e = 0; e < 2; e++) xoff[b][e] = row * 128 + (((2 * e + h) ^ ((row >> 1) & 3)) | (((row >> 1) & 4))) * 16; }
    UNR for (int a = 0; a < 2; a++) woff[a] = 16384 + (8 * h + q) * 256 + ((((wn * 2 + a) * 2 + (g & 1)) ^ (q << 1)) * 32) + p * 8;
    f32x16 acc[2][2];
    UNR for (int a = 0; a < 2; a++) UNR for (int b = 0; b < 2; b++) acc[a][b] = (f32x16){};
    bf16x8 xw[2], xx[2], yw[2] = {Z8, Z8}, yx[2] = {Z8, Z8};
#define QRD(W, X, ks) do { W[0] = cat(trrd(base + woff[0] + (ks) * 4096), trrd(base + woff[0] + (ks) * 4096 + 1024)); X[0] = rd128(base + (xoff[0][(ks) & 1] ^ (((ks) >> 1) * 64))); \
        X[1] = rd128(base + (xoff[1][(ks) & 1] ^ (((ks) >> 1) * 64))); W[1] = cat(trrd(base + woff[1] + (ks) * 4096), trrd(base + woff[1] + (ks) * 4096 + 1024)); } while (0)
#define QMM(W, X) do { UNR for (int a = 0; a < 2; a++) UNR for (int b = 0; b < 2; b++) acc[a][b] = VT ? MFMA32(X[b], W[a], acc[a][b]) : MFMA32(W[a], X[b], acc[a][b]); } while (0)
#define QSG() do { UNR for (int i = 0; i < 3; i++) { SGB(0x8, 1); SGB(0x100, 2); } SGB(0x8, 4); } while (0)
    int slot = 0;
    for (int t = 0; t < 9; t++) {
        BAR(); if (t == 0) STAMP(0); LAS const char* base = (LAS const char*)lds + slot * Q_STG;
        SB(); QRD(xw, xx, 0); QMM(yw, yx); QSG(); SB(); QRD(yw, yx, 1); QMM(xw, xx); QSG();
        SB(); QRD(xw, xx, 2); QMM(yw, yx); QSG(); SB(); QRD(yw, yx, 3); QMM(xw, xx); QSG(); SB();
        WAITL0(); slot = (slot == Q_NS - 1) ? 0 : slot + 1;
    }
    QMM(yw, yx); STAMP(1);
    BAR(); STAMP(2);
    LAS const float* mu = (LAS const float*)(lds + Q_ST); LAS const float* rs = mu + 128; LAS const float* cs = mu + 256;
    if constexpr (!VT) {
        float m_[2], r_[2];
        UNR for (int b = 0; b < 2; b++) { m_[b] = mu[wsn * 64 + b * 32 + r]; r_[b] = rs[wsn * 64 + b * 32 + r]; }
        UNR for (int a = 0; a < 2; a++) UNR for (int m = 0; m < 4; m++) {
            const int nl = wn * 64 + a * 32 + 8 * m + 4 * h, n = nt * 128 + nl; const f32x4 c4 = *(LAS const f32x4*)(cs + nl);
            const bool isk = n >= D; const int Qd = (n - (isk ? D : 0)) >> 2, ph = (Qd * 57) >> 9, qm = Qd - 9 * ph;
            const int head = 2 * ph + (qm > 4), pq = qm > 4 ? qm - 5 : qm;
            UNR for (int b = 0; b < 2; b++) {
                const int T = mt * 4 + wsn * 2 + b; const float rr = isk ? r_[b] : r_[b] * QSCALE;
                float v[4]; UNR for (int e = 0; e < 4; e++) v[e] = __builtin_fmaf(-m_[b], c4[e], acc[a][b][4 * m + e]) * rr;
                const u32x2 o = {cvtpk(v[0], v[1]), cvtpk(v[2], v[3])};
                if (qm != 4) {
                    const size_t tb = isk ? WS_KV + (size_t)head * BLOB + (T >> 3) * CHUNK + (T & 7) * 1024 : WS_QB + (size_t)(head * 64 + T) * 1024;
                    *(u32x2*)(ws + tb + (pq & 1) * 512 + (pq >> 1) * 256 + r * 8) = o;
                } else {
                    const size_t tb = isk ? WS_KV + (size_t)head * BLOB + (T >> 3) * CHUNK + 8192 + (T & 7) * 128 : WS_QR + (size_t)(head * 64 + T) * 128;
                    *(unsigned*)(ws + tb + r * 4) = o[0];
                    *(unsigned*)(ws + tb + (isk ? BLOB : 8192) + r * 4) = o[1];
                }
            }
        }
    } else {
        UNR for (int a = 0; a < 2; a++) {
            const int nl = wn * 64 + a * 32 + r, col = nt * 128 + nl - 2 * D; const float c1 = cs[nl];
            const int head = (col * 3641) >> 16, d = col - 18 * head;
            UNR for (int b = 0; b < 2; b++) {
                const int T = mt * 4 + wsn * 2 + b;
                char* tb = ws + WS_KV + (size_t)head * BLOB + (T >> 3) * CHUNK + 9216 + (T & 7) * 1216 + h * 304 + d * 16;
                UNR for (int s2 = 0; s2 < 2; s2++) {
                    u32x4 o;
                    UNR for (int e2 = 0; e2 < 2; e2++) {
                        const int m = 2 * s2 + e2, tl = wsn * 64 + b * 32 + 8 * m + 4 * h; const f32x4 m4 = *(LAS const f32x4*)(mu + tl), r4 = *(LAS const f32x4*)(rs + tl);
                        float v[4]; UNR for (int e = 0; e < 4; e++) v[e] = __builtin_fmaf(-m4[e], c1, acc[a][b][4 * m + e]) * r4[e];
                        o[2 * e2] = cvtpk(v[0], v[1]); o[2 * e2 + 1] = cvtpk(v[2], v[3]);
                    }
                    if (col < D) { *(u32x4*)(tb + s2 * 608) = o; if (d == 17) *(u32x4*)(tb + s2 * 608 + 16) = (u32x4){0x3f803f80u, 0x3f803f80u, 0x3f803f80u, 0x3f803f80u}; }
                }
            }
        }
    }
#undef QRD
#undef QMM
#undef QSG
}
__global__ __launch_bounds__(768, 3) void k_qkv(const bf16_t* __restrict__ x, const bf16_t* __restrict__ w, char* __restrict__ ws) {
    __shared__ __attribute__((aligned(16))) char lds[Q_ST + 1536]; PROFILE(0);
    const int tid = threadIdx.x, lane = tid & 63, wid = rfl(tid >> 6), bid = blockIdx.x;
    if (bid >= 224) {
        const int i = (bid - 224) * 768 + tid;
        if (i < S * 6 * 8 / 16) *(u32x4*)(ws + WS_ST + (size_t)i * 16) = (u32x4){~0u, ~0u, ~0u, ~0u};
        if (i < 256 * 128 / 16) *(u32x4*)(ws + WS_FL + (size_t)i * 16) = (u32x4){0u, 0u, 0u, 0u};
        return;
    }
    const int xg = bid & 7, idx = bid >> 3, mt = (xg >> 1) * 4 + (idx & 3), nt = (xg & 1) * 7 + (idx >> 2);
    const unsigned lds0 = (unsigned)(uintptr_t)lds;
    if (wid < 4) { if (nt >= 9) qkv_consumer<true>(lds, ws, mt, nt, wid, lane PROFPASS); else qkv_consumer<false>(lds, ws, mt, nt, wid, lane PROFPASS); }
    else if (wid < 8) {
        const int lw = wid - 4; const __amdgpu_buffer_rsrc_t sx = mksrd(x, S * D * 2), sw = mksrd(w, D * 3 * D * 2);
        unsigned vx[2];
        UNR for (int e = 0; e < 2; e++) vx[e] = (mt * 128 + 32 * lw + (lane >> 3)) * 1152 + ((lane & 7) ^ (4 * e + (lane >> 4))) * 16;
        const int rq = lane >> 4, c = lane & 15; const unsigned vw = (16 * lw + rq) * 3456 + nt * 256 + ((((c >> 1) ^ (rq << 1)) * 2) + (c & 1)) * 16;
        ring_loader<9, Q_NS, 8>([&](int t, int slot) {
            const unsigned lb = lds0 + slot * Q_STG;
            UNR for (int i = 0; i < 4; i++) dma16(lb + 16384 + (16 * lw + 4 * i) * 256, vw, sw, (t * 64 + 4 * i) * 3456);
            UNR for (int i = 0; i < 4; i++) dma16(lb + (32 * lw + 8 * i) * 128, vx[i & 1], sx, 8 * i * 1152 + t * 128);
        });
        BAR();
    } else {
        const int sw = wid - 8, r = lane & 31, h = lane >> 5, g = lane >> 4, q = (lane >> 2) & 3, p = lane & 3, row = sw * 32 + r;
        const unsigned woff = 16384 + (8 * h + q) * 256 + (((sw * 2 + (g & 1)) ^ (q << 1)) * 32) + p * 8;
        float s1 = 0.f, s2 = 0.f, c1 = 0.f; int slot = 0;
        for (int t = 0; t < 9; t++) {
            BAR(); LAS const char* base = (LAS const char*)lds + slot * Q_STG;
            UNR for (int ks = 0; ks < 4; ks++) {
                const u32x4 xv = *(LAS const u32x4*)(base + row * 128 + (((2 * ks + h) ^ ((row >> 1) & 7)) * 16));
                const u32x2 w0 = __builtin_bit_cast(u32x2, trrd(base + woff + ks * 4096)), w1 = __builtin_bit_cast(u32x2, trrd(base + woff + ks * 4096 + 1024));
                UNR for (int e = 0; e < 4; e++) { s1 = dot2(xv[e], 0x3f803f80u, s1); s2 = dot2(xv[e], xv[e], s2); }
                c1 = dot2(w0[0], 0x3f803f80u, c1); c1 = dot2(w0[1], 0x3f803f80u, c1); c1 = dot2(w1[0], 0x3f803f80u, c1); c1 = dot2(w1[1], 0x3f803f80u, c1);
            }
            WAITL0(); slot = (slot == Q_NS - 1) ? 0 : slot + 1;
        }
        s1 = swapadd32(s1); s2 = swapadd32(s2); c1 = swapadd32(c1);
        const float m = s1 * (1.f / D), var = __builtin_fmaxf(__builtin_fmaf(-m, m, s2 * (1.f / D)), 0.f);
        LAS float* st = (LAS float*)(lds + Q_ST);
        if (h == 0) { st[row] = m; st[128 + row] = __builtin_amdgcn_rsqf(var + EPS); st[256 + row] = c1; }
        WAITL0(); BAR();
    }
}

#define AT_IT(SC, SN, c0, c1, c2, c3, c4, c5, c6, c7, c8, c9, c10, c11, c12, c13, c14, c15, KO0, KO1, KR, V1, V0) \
    "s_waitcnt lgkmcnt(2)\n v_mfma_f32_32x32x16_bf16 " SN ", v[56:59], v[70:73], 0\n ds_read2st64_b64 v[56:59], v76 offset0:" KO0 " offset1:" KO1 "\n" \
    "v_exp_f32 " c0 "," c0 "\n v_exp_f32 " c1 "," c1 "\n v_exp_f32 " c2 "," c2 "\n v_exp_f32 " c3 "," c3 "\n v_cvt_pk_bf16_f32 v48," c0 "," c1 "\n v_cvt_pk_bf16_f32 v49," c2 "," c3 "\n" \
    "v_mfma_f32_32x32x8_bf16 " SN ", v[60:61], v[74:75], " SN "\n ds_read_b32 v60, v77 offset:" KR "\n" \
    "v_exp_f32 " c4 "," c4 "\n v_exp_f32 " c5 "," c5 "\n v_exp_f32 " c6 "," c6 "\n v_exp_f32 " c7 "," c7 "\n v_cvt_pk_bf16_f32 v50," c4 "," c5 "\n v_cvt_pk_bf16_f32 v51," c6 "," c7 "\n" \
    "s_waitcnt lgkmcnt(2)\n v_mfma_f32_32x32x16_bf16 v[32:47], v[66:69], v[52:55], v[32:47]\n" \
    "v_exp_f32 " c8 "," c8 "\n v_exp_f32 " c9 "," c9 "\n v_exp_f32 " c10 "," c10 "\n v_exp_f32 " c11 "," c11 "\n v_cvt_pk_bf16_f32 v52," c8 "," c9 "\n v_cvt_pk_bf16_f32 v53," c10 "," c11 "\n" \
    "v_mfma_f32_32x32x16_bf16 v[32:47], v[62:65], v[48:51], v[32:47]\n" \
    "s_mov_b64 exec, s[36:37]\n ds_read_b128 v[66:69], v78 offset:" V1 "\n ds_read_b128 v[62:65], v78 offset:" V0 "\n s_mov_b64 exec, -1\n" \
    "v_exp_f32 " c12 "," c12 "\n v_exp_f32 " c13 "," c13 "\n v_exp_f32 " c14 "," c14 "\n v_exp_f32 " c15 "," c15 "\n v_cvt_pk_bf16_f32 v54," c12 "," c13 "\n v_cvt_pk_bf16_f32 v55," c14 "," c15 "\n"
#define AT_A(KO0, KO1, KR, V1, V0) AT_IT("v[0:15]", "v[16:31]", "v0", "v1", "v2", "v3", "v4", "v5", "v6", "v7", "v8", "v9", "v10", "v11", "v12", "v13", "v14", "v15", KO0, KO1, KR, V1, V0)
#define AT_B(KO0, KO1, KR, V1, V0) AT_IT("v[16:31]", "v[0:15]", "v16", "v17", "v18", "v19", "v20", "v21", "v22", "v23", "v24", "v25", "v26", "v27", "v28", "v29", "v30", "v31", KO0, KO1, KR, V1, V0)
#define AT_SWAP 6
__global__ __launch_bounds__(576, 3) void k_attn(char* __restrict__ ws, const bf16_t* __restrict__ wo, const bf16_t* __restrict__ w1, const bf16_t* __restrict__ w2) {
    __shared__ __attribute__((aligned(16))) char lds[BLOB + 10240]; PROFILE(1);
    const int tid = threadIdx.x, lane = tid & 63, wid = rfl(tid >> 6), bid = blockIdx.x;
    const int head = (bid & 7) * 4 + ((bid >> 3) & 3), qp = bid >> 5;
    const unsigned lds0 = (unsigned)(uintptr_t)lds;
    const __amdgpu_buffer_rsrc_t skv = mksrd(ws + WS_KV + (size_t)head * BLOB, BLOB);
    const unsigned v16 = lane * 16;
    if (wid == 8) {
        for (int pc = 19; pc < 60; pc++) dma16(lds0 + pc * 1024, v16, skv, pc * 1024);
        BAR();
        for (int pc = 60; pc < 148; pc += 4) { waitv<40>(); UNR for (int i = 0; i < 4; i++) dma16(lds0 + (pc + i) * 1024, v16, skv, (pc + i) * 1024); }
        waitv<0>(); BAR();
        { unsigned sink = 0; const int xg = bid & 7, i = bid >> 3; const __amdgpu_buffer_rsrc_t s0 = mksrd(wo, D * D * 2), s1 = mksrd(w1, S * F * 2), s2 = mksrd(w2, S * F * 2);
          for (int k = 0; k < 9; k++) touch(sink, ((xg >> 1) * 1152 + 36 * i + (lane >> 4)) * (S * 2) + (xg & 1) * 2048 + (lane & 15) * 128, s2, k * 4 * (S * 2));
          for (int k = 0; k < 10; k++) touch(sink, (64 * i + lane) * (F * 2) + ((9 * xg) >> 1) * 256, s1, k * 128);
          for (int k = 0; k < 3; k++) touch(sink, (i * 162 + lane) * 128, s0, k * 8192);
          asm volatile("s_waitcnt vmcnt(0)" : "+v"(sink) :: "memory"); }
        return;
    }
    dma16(lds0 + wid * 1024, v16, skv, wid * 1024); dma16(lds0 + (wid + 8) * 1024, v16, skv, (wid + 8) * 1024);
    if (wid < 3) dma16(lds0 + (wid + 16) * 1024, v16, skv, (wid + 16) * 1024);
    const int r = lane & 31, h = lane >> 5, T = qp * 8 + wid;
    const char* qb = ws + WS_QB + (size_t)(head * 64 + T) * 1024 + h * 256 + r * 8;
    const u32x2 q0 = *(const u32x2*)qb, q1 = *(const u32x2*)(qb + 512);
    const unsigned qrest = *(const unsigned*)(ws + WS_QR + (size_t)(head * 64 + T) * 128 + r * 4);
    const bf16x8 qm = __builtin_bit_cast(bf16x8, (u32x4){q0[0], q0[1], q1[0], q1[1]});
    u32x2 qr = {h ? 0u : qrest, 0u};
    waitv<0>(); STAMP(0); BAR(); STAMP(1);
    const unsigned ka = h * 256 + r * 8, kra = 8192 + r * 4, va = 9216 + h * 304 + r * 16;
    {
        LAS const char* b = (LAS const char*)lds;
        const u32x2 k0 = *(LAS const u32x2*)(b + ka), k1 = *(LAS const u32x2*)(b + ka + 512); const u32x2 kr = {*(LAS const unsigned*)(b + kra), 0u};
        f32x16 s = MFMA32(__builtin_bit_cast(bf16x8, (u32x4){k0[0], k0[1], k1[0], k1[1]}), qm, (f32x16){});
        s = MFMA32K8(__builtin_bit_cast(s16x4, kr), __builtin_bit_cast(s16x4, qr), s);
        float m = s[0]; UNR for (int i = 1; i < 16; i++) m = __builtin_fmaxf(m, s[i]);
        m = swapmax32(m);
        qr[1] = h ? 0u : (cvtpk(-m, 0.f) & 0xffffu);
    }
    f32x16 O; STAMP(2);
    {
        unsigned a0 = lds0 + ka, a1 = lds0 + kra, a2 = lds0 + va;
        asm volatile(
            "s_mov_b32 s36, 0x7ffff\n s_mov_b32 s37, 0x7ffff\n s_mov_b32 s38, 0\n"
            "s_setprio 1\n s_cmp_ge_u32 %[wid], 4\n s_cbranch_scc1 1f\n s_setprio 2\n 1:\n"
            "ds_read2st64_b64 v[56:59], v76 offset0:0 offset1:1\n ds_read_b32 v60, v77\n"
            "v_mov_b32 v61, 0x3f80\n"
            "v_mov_b32 v32, 0\n v_mov_b32 v33, 0\n v_mov_b32 v34, 0\n v_mov_b32 v35, 0\n v_mov_b32 v36, 0\n v_mov_b32 v37, 0\n v_mov_b32 v38, 0\n v_mov_b32 v39, 0\n"
            "v_mov_b32 v40, 0\n v_mov_b32 v41, 0\n v_mov_b32 v42, 0\n v_mov_b32 v43, 0\n v_mov_b32 v44, 0\n v_mov_b32 v45, 0\n v_mov_b32 v46, 0\n v_mov_b32 v47, 0\n"
            "v_mov_b32 v52, 0\n v_mov_b32 v53, 0\n v_mov_b32 v54, 0\n v_mov_b32 v55, 0\n"
            "v_mov_b32 v62, 0\n v_mov_b32 v63, 0\n v_mov_b32 v64, 0\n v_mov_b32 v65, 0\n v_mov_b32 v66, 0\n v_mov_b32 v67, 0\n v_mov_b32 v68, 0\n v_mov_b32 v69, 0\n"
            "s_waitcnt lgkmcnt(0)\n"
            "v_mfma_f32_32x32x16_bf16 v[0:15], v[56:59], v[70:73], 0\n v_mfma_f32_32x32x8_bf16 v[0:15], v[60:61], v[74:75], v[0:15]\n"
            "ds_read2st64_b64 v[56:59], v76 offset0:2 offset1:3\n ds_read_b32 v60, v77 offset:128\n"
            "s_mov_b64 exec, s[36:37]\n ds_read_b128 v[66:69], v78 offset:608\n ds_read_b128 v[62:65], v78\n s_mov_b64 exec, -1\n"
            "s_nop 7\n s_nop 7\n"
            "2:\n"
            AT_A("4", "5", "256", "608", "1216") AT_B("6", "7", "384", "1824", "2432") AT_A("8", "9", "512", "3040", "3648") AT_B("10", "11", "640", "4256", "4864")
            AT_A("12", "13", "768", "5472", "6080") AT_B("14", "15", "896", "6688", "7296")
            "s_cmp_lg_u32 s38, 0\n s_cbranch_scc1 3f\n s_barrier\n 3:\n"
            AT_A("37", "38", "18944", "7904", "8512") AT_B("39", "40", "19072", "9120", "18944")
            "v_add_u32 v76, 0x4a00, v76\n v_add_u32 v77, 0x4a00, v77\n v_add_u32 v78, 0x4a00, v78\n"
            "s_add_u32 s38, s38, 1\n s_cmp_lg_u32 s38, %[swap]\n s_cbranch_scc1 4f\n s_cmp_ge_u32 %[wid], 4\n s_cbranch_scc1 4f\n s_setprio 0\n 4:\n"
            "s_cmp_lt_u32 s38, 8\n s_cbranch_scc1 2b\n"
            "s_waitcnt lgkmcnt(0)\n v_mfma_f32_32x32x16_bf16 v[32:47], v[66:69], v[52:55], v[32:47]\n s_setprio 0\n s_nop 7\n s_nop 7\n"
            : "={v[32:47]}"(O), "+{v76}"(a0), "+{v77}"(a1), "+{v78}"(a2)
            : "{v[70:73]}"(qm), "{v[74:75]}"(qr), [wid] "s"(wid), [swap] "i"(AT_SWAP)
            : "memory", "scc", "s36", "s37", "s38", "v0", "v1", "v2", "v3", "v4", "v5", "v6", "v7", "v8", "v9", "v10", "v11", "v12", "v13", "v14", "v15", "v16", "v17", "v18", "v19", "v20", "v21", "v22", "v23",
              "v24", "v25", "v26", "v27", "v28", "v29", "v30", "v31", "v48", "v49", "v50", "v51", "v52", "v53", "v54", "v55", "v56", "v57", "v58", "v59", "v60", "v61", "v62", "v63", "v64", "v65", "v66", "v67", "v68", "v69");
    }
    STAMP(3);
    float l = __shfl(O[10], r);
    const bool bad = !(l < 1.8e19f && l > 1e-18f);
    if (__builtin_expect(__any(bad), 0)) {
        LAS const char* b = (LAS const char*)lds; qr[1] = 0u; float m = -INFINITY;
        const unsigned vs = 9216 + h * 304 + (r < 19 ? r : r - 16) * 16;
        for (int pass = 0; pass < 2; pass++) {
            O = (f32x16){};
            for (int t = 0; t < 64; t++) {
                LAS const char* cb = b + (t >> 3) * CHUNK; const int i = t & 7;
                const u32x2 k0 = *(LAS const u32x2*)(cb + i * 1024 + ka), k1 = *(LAS const u32x2*)(cb + i * 1024 + ka + 512); const u32x2 kr = {*(LAS const unsigned*)(cb + i * 128 + kra), 0u};
                f32x16 s = MFMA32(__builtin_bit_cast(bf16x8, (u32x4){k0[0], k0[1], k1[0], k1[1]}), qm, (f32x16){});
                s = MFMA32K8(__builtin_bit_cast(s16x4, kr), __builtin_bit_cast(s16x4, qr), s);
                if (pass == 0) { UNR for (int e = 0; e < 16; e++) m = __builtin_fmaxf(m, s[e]); }
                else {
                    u32x4 p0, p1;
                    UNR for (int e = 0; e < 4; e++) { p0[e] = cvtpk(__builtin_amdgcn_exp2f(s[2 * e] - m), __builtin_amdgcn_exp2f(s[2 * e + 1] - m)); p1[e] = cvtpk(__builtin_amdgcn_exp2f(s[8 + 2 * e] - m), __builtin_amdgcn_exp2f(s[9 + 2 * e] - m)); }
                    asm volatile("s_nop 1" : "+v"(p0), "+v"(p1));
                    O = MFMA32(rd128(cb + i * 1216 + vs), __builtin_bit_cast(bf16x8, p0), O); O = MFMA32(rd128(cb + i * 1216 + 608 + vs), __builtin_bit_cast(bf16x8, p1), O);
                }
            }
            m = swapmax32(m);
        }
        l = __shfl(O[10], r);
    }
    const float il = 1.f / l; const int qrow = T * 32 + r;
    char* of = ws + WS_OF + (size_t)(qrow >> 4) * 18432;
    *(u32x4*)(of + (head >> 1) * 1024 + ((2 * (head & 1) + h) * 16 + (r & 15)) * 16) = (u32x4){cvtpk(O[0] * il, O[1] * il), cvtpk(O[2] * il, O[3] * il), cvtpk(O[4] * il, O[5] * il), cvtpk(O[6] * il, O[7] * il)};
    if (h == 0) *(unsigned*)(of + (16 + (head >> 4)) * 1024 + (((head & 15) >> 2) * 16 + (r & 15)) * 16 + (head & 3) * 4) = cvtpk(O[8] * il, O[9] * il);
}

constexpr int O_X = 9 * 12288, O_T = O_X + 12288;
__global__ __launch_bounds__(512, 2) void k_wo(const bf16_t* __restrict__ x, const bf16_t* __restrict__ wo, char* __restrict__ ws) {
    __shared__ __attribute__((aligned(16))) char lds[O_T + 4 * 3328]; PROFILE(2);
    const int tid = threadIdx.x, lane = tid & 63, wid = rfl(tid >> 6), bid = blockIdx.x;
    const int idx = bid >> 3, st = (bid & 7) * 4 + idx / 6, nt = idx % 6;
    const unsigned lds0 = (unsigned)(uintptr_t)lds;
    if (wid >= 4) {
        const int lw = wid - 4; const __amdgpu_buffer_rsrc_t sw = mksrd(wo, D * D * 2), sx = mksrd(x, S * D * 2);
        unsigned vm[3], vr[3], vx[3];
        UNR for (int i = 0; i < 3; i++) {
            const int c = i * 64 + lane, rl = c / 12, cc = c - 12 * rl, rho = 16 * lw + rl, g = (rho >> 2) & 3, j = (rho & 3) + 4 * ((rho >> 4) & 1), kb = rho >> 5;
            const int col = nt * 192 + ((((cc >> 1) ^ (g & 1)) * 2) + (cc & 1)) * 16;
            vm[i] = (36 * kb + 18 * (g >> 1) + 4 * (g & 1) + (j & 3) + 8 * (j >> 2)) * 1152 + col;
            vr[i] = (18 * (16 * kb + 4 * g + (j >> 1)) + 16 + (j & 1)) * 1152 + col;
            vx[i] = (st * 64 + rho) * 1152 + nt * 192 + cc * 16;
        }
        auto issue = [&](int t) {
            UNR for (int i = 0; i < 3; i++) {
                if (t < 8) dma16(lds0 + t * 12288 + (3 * lw + i) * 1024, vm[i], sw, t * 72 * 1152);
                else if (t == 8) dma16(lds0 + t * 12288 + (3 * lw + i) * 1024, vr[i], sw, 0);
                else dma16(lds0 + O_X + (3 * lw + i) * 1024, vx[i], sx, 0);
            } };
        issue(0); issue(1); waitv<3>(); BAR(); issue(2); issue(3); issue(4);
        UNR for (int t = 1; t <= 5; t++) { waitv<9>(); BAR(); issue(t + 4); }
        waitv<9>(); BAR(); waitv<6>(); BAR(); waitv<3>(); BAR(); waitv<0>(); BAR();
        return;
    }
    const int s = lane & 15, g = lane >> 4, q = (lane >> 2) & 3, p = lane & 3, s16 = st * 4 + wid;
    bf16x8 of[18];
    UNR for (int b = 0; b < 18; b++) of[b] = *(const bf16x8*)(ws + WS_OF + (size_t)s16 * 18432 + b * 1024 + lane * 16);
    const unsigned be = (4 * g + q) * 192 + (g & 1) * 32 + 8 * p, bo = (4 * g + q) * 192 - (g & 1) * 32 + 8 * p;
    f32x4 acc[6];
    UNR for (int t = 0; t < 6; t++) acc[t] = (f32x4){0.f, 0.f, 0.f, 0.f};
    UNR for (int t = 0; t < 9; t++) {
        BAR(); if (t == 0) STAMP(0); LAS const char* base = (LAS const char*)lds + t * 12288;
        UNR for (int kb = 0; kb < 2; kb++) UNR for (int n = 0; n < 6; n++) {
            LAS const char* a = base + kb * 6144 + ((n & 1) ? bo : be) + n * 32;
            acc[n] = MFMA16(cat(trrd(a), trrd(a + 3072)), of[2 * t + kb], acc[n]);
        }
    }
    STAMP(1); BAR(); STAMP(2);
    float hv[6][4], s1 = 0.f, s2 = 0.f;
    char* hf = ws + WS_HF + (size_t)s16 * 18432;
    UNR for (int n = 0; n < 6; n++) {
        const u32x2 xv = *(LAS const u32x2*)((LAS const char*)lds + O_X + (wid * 16 + s) * 192 + n * 32 + g * 8);
        const unsigned a0 = cvtpk(acc[n][0], acc[n][1]), a1 = cvtpk(acc[n][2], acc[n][3]);
        const u32x2 o = {cvtpk(blo(xv[0]) + blo(a0), bhi(xv[0]) + bhi(a0)), cvtpk(blo(xv[1]) + blo(a1), bhi(xv[1]) + bhi(a1))};
        hv[n][0] = blo(o[0]); hv[n][1] = bhi(o[0]); hv[n][2] = blo(o[1]); hv[n][3] = bhi(o[1]);
        UNR for (int e = 0; e < 4; e++) { s1 += hv[n][e]; s2 = __builtin_fmaf(hv[n][e], hv[n][e], s2); }
        const int t16 = nt * 6 + n, dT = t16 / 9, tl = t16 - 9 * dT;
        const unsigned off = tl < 8 ? (tl >> 1) * 1024 + ((2 * (tl & 1) + (g >> 1)) * 16 + s) * 16 + (g & 1) * 8 : 4096 + lane * 8;
        *(u32x2*)(hf + dT * 4608 + off) = o;
    }
    s1 = swapadd32(swapadd16(s1)); s2 = swapadd32(swapadd16(s2));
    gu64* gs = (gu64*)(ws + WS_ST) + (size_t)(s16 * 16 + s) * 6;
    { unsigned b2 = __float_as_uint(s2); if (b2 == ~0u) b2 = 0x7fc00000u;
      if (g == 0) __hip_atomic_store(gs + nt, ((unsigned long long)b2 << 32) | __float_as_uint(s1), RLX_AGENT); }
    STAMP(3);
    unsigned long long w0, w1 = 0;
    for (unsigned spins = 0;; spins++) {
        w0 = __hip_atomic_load(gs + g, RLX_AGENT); if (g < 2) w1 = __hip_atomic_load(gs + g + 4, RLX_AGENT);
        if (__all((unsigned)(w0 >> 32) != ~0u && (unsigned)(w1 >> 32) != ~0u) || spins > SPIN_CAP) break;
        __builtin_amdgcn_s_sleep(1);
    }
    STAMP(4);
    const float t1 = swapadd32(swapadd16(__uint_as_float((unsigned)w0) + __uint_as_float((unsigned)w1)));
    const float t2 = swapadd32(swapadd16(__uint_as_float((unsigned)(w0 >> 32)) + __uint_as_float((unsigned)(w1 >> 32))));
    const float m = t1 * (1.f / D), var = __builtin_fmaxf(__builtin_fmaf(-m, m, t2 * (1.f / D)), 0.f);
    const float r1 = __builtin_amdgcn_rsqf(var + EPS), rr = r1 * __builtin_amdgcn_rsqf(var * r1 * r1 + EPS);
    LAS char* tb = (LAS char*)lds + O_T + wid * 3328;
    UNR for (int n = 0; n < 6; n++) *(LAS u32x2*)(tb + s * 208 + n * 32 + g * 8) = (u32x2){cvtpk((hv[n][0] - m) * rr, (hv[n][1] - m) * rr), cvtpk((hv[n][2] - m) * rr, (hv[n][3] - m) * rr)};
    WAITL0();
    UNR for (int dt = 0; dt < 3; dt++) {
        LAS const char* a = tb + (4 * (g >> 1) + q) * 208 + dt * 64 + (g & 1) * 32 + p * 8;
        *(bf16x8*)(ws + WS_HH + (size_t)((nt * 128 + s16) * 3 + dt) * 1024 + lane * 16) = cat(trrd(a), trrd(a + 8 * 208));
    }
}

constexpr int A_STG = 28 * 1024, A_NS = 5;
__global__ __launch_bounds__(512, 2) void k_up(const bf16_t* __restrict__ w1, const bf16_t* __restrict__ b1, char* __restrict__ ws) {
    __shared__ __attribute__((aligned(16))) char lds[A_NS * A_STG]; PROFILE(3);
    const int tid = threadIdx.x, lane = tid & 63, wid = rfl(tid >> 6), bid = blockIdx.x;
    const int L = (bid & 7) * 27 + (bid >> 3), ft = L / 6, dT = L % 6;
    const unsigned lds0 = (unsigned)(uintptr_t)lds;
    if (wid >= 4) {
        const int lw = wid - 4, rq = lane >> 4, c = lane & 15; const __amdgpu_buffer_rsrc_t sw = mksrd(w1, S * F * 2), sh = mksrd(ws + WS_HH, S * D * 2);
        const unsigned vw = (16 * lw + rq) * (F * 2) + ft * 256 + ((((c >> 1) ^ (rq << 1)) * 2) + (c & 1)) * 16, vh = lane * 16;
        ring_loader<32, A_NS, 7>([&](int t, int slot) {
            const unsigned lb = lds0 + slot * A_STG;
            UNR for (int i = 0; i < 4; i++) dma16(lb + (16 * lw + 4 * i) * 256, vw, sw, (t * 64 + 4 * i) * (F * 2));
            UNR for (int j = 0; j < 3; j++) dma16(lb + 16384 + (lw * 3 + j) * 1024, vh, sh, (dT * 32 + t) * 12288 + (lw * 3 + j) * 1024);
        });
        BAR(); BAR();
        return;
    }
    const int wf = wid & 1, kh = wid >> 1, r = lane & 31, h = lane >> 5, g = lane >> 4, q = (lane >> 2) & 3, p = lane & 3;
    const int f0 = ft * 128 + (wf * 2 + kh) * 32;
    u32x2 bb[4];
    UNR for (int m = 0; m < 4; m++) bb[m] = *(const u32x2*)(b1 + f0 + 8 * m + 4 * h);
    unsigned aoff[2];
    UNR for (int i = 0; i < 2; i++) aoff[i] = kh * 8192 + (4 * h + q) * 256 + ((((wf * 2 + i) * 2 + (g & 1)) ^ (q << 1)) * 32) + p * 8;
    const unsigned boff = 16384 + kh * 6144 + lane * 16;
    f32x16 acc[2][3];
    UNR for (int i = 0; i < 2; i++) UNR for (int j = 0; j < 3; j++) acc[i][j] = (f32x16){};
    bf16x8 xa[2], xb[3], ya[2] = {Z8, Z8}, yb[3] = {Z8, Z8, Z8};
#define ARD(A, B, ks) do { B[0] = rd128(base + boff + (ks) * 3072); A[0] = cat(trrd(base + aoff[0] + (ks) * 4096), trrd(base + aoff[0] + (ks) * 4096 + 2048)); B[1] = rd128(base + boff + (ks) * 3072 + 1024); \
        B[2] = rd128(base + boff + (ks) * 3072 + 2048); A[1] = cat(trrd(base + aoff[1] + (ks) * 4096), trrd(base + aoff[1] + (ks) * 4096 + 2048)); } while (0)
#define AMM(A, B) do { UNR for (int i = 0; i < 2; i++) UNR for (int j = 0; j < 3; j++) acc[i][j] = MFMA32(A[i], B[j], acc[i][j]); } while (0)
#define ASG() do { UNR for (int i = 0; i < 3; i++) { SGB(0x8, 1); SGB(0x100, 3); } SGB(0x8, 6); } while (0)
    int slot = 0;
    for (int t = 0; t < 32; t++) {
        BAR(); if (t == 0) STAMP(0); LAS const char* base = (LAS const char*)lds + slot * A_STG;
        SB(); ARD(xa, xb, 0); AMM(ya, yb); ASG(); SB(); ARD(ya, yb, 1); AMM(xa, xb); ASG(); SB();
        WAITL0(); slot = (slot == A_NS - 1) ? 0 : slot + 1;
    }
    AMM(ya, yb); STAMP(1);
    BAR();
    LAS f32x4* ex = (LAS f32x4*)lds + wid * 12 * 64 + lane;
    UNR for (int j = 0; j < 3; j++) UNR for (int m = 0; m < 4; m++) {
        f32x4 v; UNR for (int e = 0; e < 4; e++) v[e] = kh ? acc[0][j][4 * m + e] : acc[1][j][4 * m + e];
        ex[(j * 4 + m) * 64] = v; }
    WAITL0(); BAR();
    STAMP(2);
    LAS const f32x4* ey = (LAS const f32x4*)lds + (wid ^ 2) * 12 * 64 + lane;
    UNR for (int j = 0; j < 3; j++) {
        const int d = dT * 96 + j * 32 + r, dT9 = d / 144, dl = d - 144 * dT9;
        const int tl = dl < 128 ? 2 * (dl >> 5) + ((dl >> 2) & 1) : 8, rho = dl < 128 ? 4 * ((dl >> 3) & 3) + (dl & 3) : dl - 128;
        char* ub = ws + WS_UF + (size_t)((dT9 * 144 + (f0 >> 5)) * 9 + tl) * 1024 + (h * 16 + rho) * 16;
        UNR for (int s2 = 0; s2 < 2; s2++) {
            u32x4 o;
            UNR for (int e2 = 0; e2 < 2; e2++) {
                const int m = 2 * s2 + e2; const f32x4 y = ey[(j * 4 + m) * 64]; float v[4];
                UNR for (int e = 0; e < 4; e++) v[e] = y[e] + (kh ? acc[1][j][4 * m + e] : acc[0][j][4 * m + e]);
                o[2 * e2] = cvtpk(gelu1(v[0] + blo(bb[m][0])), gelu1(v[1] + bhi(bb[m][0]))); o[2 * e2 + 1] = cvtpk(gelu1(v[2] + blo(bb[m][1])), gelu1(v[3] + bhi(bb[m][1])));
            }
            *(u32x4*)(ub + s2 * 512) = o;
        }
    }
#undef ARD
#undef AMM
#undef ASG
}

constexpr int B_STG = 34 * 1024, B_NS = 4;
__global__ __launch_bounds__(512, 2) void k_down(const bf16_t* __restrict__ w2, const bf16_t* __restrict__ b2, char* __restrict__ ws, bf16_t* __restrict__ out) {
    __shared__ __attribute__((aligned(16))) char lds[B_NS * B_STG]; PROFILE(4);
    const int tid = threadIdx.x, lane = tid & 63, wid = rfl(tid >> 6), bid = blockIdx.x;
    const int xg = bid & 7, idx = bid >> 3, kq = xg >> 1, st = (xg & 1) * 8 + (idx >> 2), dT = idx & 3, tile = st * 4 + dT;
    const unsigned lds0 = (unsigned)(uintptr_t)lds;
    if (wid >= 4) {
        const int lw = wid - 4, rq = lane >> 4, c = lane & 15; const __amdgpu_buffer_rsrc_t sw = mksrd(w2, S * F * 2), su = mksrd(ws + WS_UF, D * F * 2);
        unsigned vw[2];
        UNR for (int e = 0; e < 2; e++) vw[e] = (kq * 1152 + 16 * lw + rq) * (S * 2) + st * 256 + ((((c >> 1) ^ (4 * e + rq)) * 2) + (c & 1)) * 16;
        const unsigned vu = lane * 16;
        const int u0 = lw < 2 ? lw * 5 : 10 + (lw - 2) * 4, ul = lw < 2 ? 4 : 3;
        ring_loader<18, B_NS, 9>([&](int t, int slot) {
            const unsigned lb = lds0 + slot * B_STG;
            UNR for (int i = 0; i < 4; i++) dma16(lb + (16 * lw + 4 * i) * 256, vw[i & 1], sw, (t * 64 + 4 * i) * (S * 2));
            UNR for (int j = 0; j < 5; j++) { const int pc = u0 + (j < 4 ? j : ul); dma16(lb + 16384 + pc * 1024, vu, su, (dT * 144 + kq * 36 + 2 * t) * 9216 + pc * 1024); }
        });
        return;
    }
    const int s = lane & 15, g = lane >> 4, q = (lane >> 2) & 3, p = lane & 3, r7 = 4 * (g & 1) + q;
    unsigned boff[2];
    UNR for (int j = 0; j < 2; j++) boff[j] = (16 * (g >> 1) + r7) * 256 + (((wid * 2 + j) ^ r7) * 32) + p * 8;
    const unsigned aoff = 16384 + lane * 16;
    f32x4 acc[9][2];
    UNR for (int i = 0; i < 9; i++) UNR for (int j = 0; j < 2; j++) acc[i][j] = (f32x4){0.f, 0.f, 0.f, 0.f};
    bf16x8 xa[9], xb[2], ya[9], yb[2] = {Z8, Z8};
    UNR for (int i = 0; i < 9; i++) ya[i] = Z8;
#define BRD(A, B, ks) do { B[0] = cat(trrd(base + boff[0] + (ks) * 8192), trrd(base + boff[0] + (ks) * 8192 + 2048)); A[0] = rd128(base + aoff + (ks) * 9216); \
        B[1] = cat(trrd(base + boff[1] + (ks) * 8192), trrd(base + boff[1] + (ks) * 8192 + 2048)); UNR for (int i = 1; i < 9; i++) A[i] = rd128(base + aoff + (ks) * 9216 + i * 1024); } while (0)
#define BMM(A, B) do { UNR for (int i = 0; i < 9; i++) UNR for (int j = 0; j < 2; j++) acc[i][j] = MFMA16(A[i], B[j], acc[i][j]); } while (0)
#define BSG() do { UNR for (int i = 0; i < 13; i++) { SGB(0x8, 1); SGB(0x100, 1); } SGB(0x8, 18); } while (0)
    int slot = 0;
    for (int t = 0; t < 18; t++) {
        BAR(); if (t == 0) STAMP(0); LAS const char* base = (LAS const char*)lds + slot * B_STG;
        SB(); BRD(xa, xb, 0); BMM(ya, yb); BSG(); SB(); BRD(ya, yb, 1); BMM(xa, xb); BSG(); SB();
        WAITL0(); slot = (slot == B_NS - 1) ? 0 : slot + 1;
    }
    BMM(ya, yb); STAMP(1);
    const __amdgpu_buffer_rsrc_t sp = mksrd(ws + WS_PX, 64 * 4 * 3 * 9216);
    gu32* fl = (gu32*)(ws + WS_FL) + (tile * 4 + wid) * 32;
    if (wid != kq) {
        const unsigned o = ((tile * 4 + wid) * 3 + (kq > wid ? kq - 1 : kq)) * 9216;
        UNR for (int i = 0; i < 9; i++)
            __builtin_amdgcn_raw_buffer_store_b128((u32x4){cvtpk(acc[i][0][0], acc[i][0][1]), cvtpk(acc[i][0][2], acc[i][0][3]), cvtpk(acc[i][1][0], acc[i][1][1]), cvtpk(acc[i][1][2], acc[i][1][3])}, sp, lane * 16 + i * 1024, o, 16);
        STAMP(2); waitv<0>(); STAMP(3);
        if (lane == 0) __hip_atomic_fetch_add(fl, 1u, RLX_AGENT);
        return;
    }
    u32x4 hp[2][4]; u32x2 hs[2]; float bs[2];
    UNR for (int j = 0; j < 2; j++) {
        const int s16 = st * 8 + wid * 2 + j; const char* hf = ws + WS_HF + (size_t)s16 * 18432 + dT * 4608;
        UNR for (int i = 0; i < 4; i++) hp[j][i] = *(const u32x4*)(hf + i * 1024 + lane * 16);
        hs[j] = *(const u32x2*)(hf + 4096 + lane * 8); bs[j] = blo(b2[s16 * 16 + s]);
    }
    STAMP(2);
    for (unsigned spins = 0; rfl(__hip_atomic_load(fl, RLX_AGENT)) < 3u && spins < SPIN_CAP; spins++) __builtin_amdgcn_s_sleep(1);
    STAMP(3);
    __builtin_amdgcn_fence(__ATOMIC_ACQUIRE, "wavefront");
    UNR for (int c = 0; c < 3; c++) UNR for (int i = 0; i < 9; i++) {
        const u32x4 v = __builtin_amdgcn_raw_buffer_load_b128(sp, lane * 16 + i * 1024, ((tile * 4 + wid) * 3 + c) * 9216, 16);
        acc[i][0][0] += blo(v[0]); acc[i][0][1] += bhi(v[0]); acc[i][0][2] += blo(v[1]); acc[i][0][3] += bhi(v[1]);
        acc[i][1][0] += blo(v[2]); acc[i][1][1] += bhi(v[2]); acc[i][1][2] += blo(v[3]); acc[i][1][3] += bhi(v[3]);
    }
    STAMP(4);
    UNR for (int j = 0; j < 2; j++) {
        bf16_t* orow = out + (size_t)(st * 128 + wid * 32 + j * 16 + s) * D + dT * 144;
        UNR for (int i = 0; i < 4; i++) {
            const f32x4 a = acc[2 * i][j], b = acc[2 * i + 1][j]; const u32x4 hh = hp[j][i];
            *(u32x4*)(orow + 32 * i + 8 * g) = (u32x4){cvtpk(a[0] + bs[j] + blo(hh[0]), a[1] + bs[j] + bhi(hh[0])), cvtpk(a[2] + bs[j] + blo(hh[1]), a[3] + bs[j] + bhi(hh[1])),
                                                       cvtpk(b[0] + bs[j] + blo(hh[2]), b[1] + bs[j] + bhi(hh[2])), cvtpk(b[2] + bs[j] + blo(hh[3]), b[3] + bs[j] + bhi(hh[3]))};
        }
        const f32x4 a = acc[8][j];
        *(u32x2*)(orow + 128 + 4 * g) = (u32x2){cvtpk(a[0] + bs[j] + blo(hs[j][0]), a[1] + bs[j] + bhi(hs[j][0])), cvtpk(a[2] + bs[j] + blo(hs[j][1]), a[3] + bs[j] + bhi(hs[j][1]))};
    }
#undef BRD
#undef BMM
#undef BSG
}

extern "C" void kernel_launch(void* const* d_in, const int* in_sizes, int n_in, void* d_out, int out_size, void* d_ws, size_t ws_size, hipStream_t stream) {
    const bf16_t* x = (const bf16_t*)d_in[0]; const bf16_t* wqkv = (const bf16_t*)d_in[1]; const bf16_t* wo = (const bf16_t*)d_in[2];
    const bf16_t* w1 = (const bf16_t*)d_in[4]; const bf16_t* b1 = (const bf16_t*)d_in[5]; const bf16_t* w2 = (const bf16_t*)d_in[6]; const bf16_t* b2 = (const bf16_t*)d_in[7];
    char* ws = (char*)d_ws;
    k_qkv<<<256, 768, 0, stream>>>(x, wqkv, ws);
    k_attn<<<256, 576, 0, stream>>>(ws, wo, w1, w2);
    k_wo<<<192, 512, 0, stream>>>(x, wo, ws);
    k_up<<<216, 512, 0, stream>>>(w1, b1, ws);
    k_down<<<256, 512, 0, stream>>>(w2, b2, ws, (bf16_t*)d_out);
}
```

```cpp
#include <hip/hip_runtime.h>
#include <stdint.h>
#include <type_traits>

typedef unsigned short bf16_t;
typedef short bf16x8 __attribute__((ext_vector_type(8)));
typedef short s16x4 __attribute__((ext_vector_type(4)));
typedef float f32x2 __attribute__((ext_vector_type(2)));
typedef float f32x4 __attribute__((ext_vector_type(4)));
typedef float f32x16 __attribute__((ext_vector_type(16)));
typedef unsigned u32x2 __attribute__((ext_vector_type(2)));
typedef unsigned u32x4 __attribute__((ext_vector_type(4)));
typedef __attribute__((address_space(1))) unsigned long long gu64;
typedef __attribute__((address_space(1))) unsigned gu32;
typedef __amdgpu_buffer_rsrc_t srd_t;
#define LAS __attribute__((address_space(3)))
#define DI __device__ __forceinline__
#define RLX_AGENT __ATOMIC_RELAXED, __HIP_MEMORY_SCOPE_AGENT

constexpr int S = 2048, D = 576, F = 4608;
constexpr int CHUNK = 18944, BLOB = 8 * CHUNK;
constexpr unsigned WS_QB = 0;
constexpr unsigned WS_QR = WS_QB + 32 * 64 * 1024;
constexpr unsigned WS_KV = WS_QR + 32 * 2048 * 4;
constexpr unsigned WS_OF = WS_KV + 32 * BLOB;
constexpr unsigned WS_HF = WS_OF + S * D * 2;
constexpr unsigned WS_HH = WS_HF + S * D * 2;
constexpr unsigned WS_UF = WS_HH + S * D * 2;
constexpr unsigned WS_ST = WS_UF + D * F * 2;
constexpr unsigned WS_F2 = WS_ST + S * 6 * 8;
constexpr unsigned WS_F3 = WS_F2 + 8192;
constexpr unsigned WS_F4 = WS_F3 + 3072;
constexpr unsigned WS_F5 = WS_F4 + 4096;
constexpr unsigned WS_F1 = WS_F5 + 256 * 128;
constexpr unsigned WS_EP = WS_F1 + 2048;
constexpr unsigned WS_PX = WS_EP + 128;
constexpr unsigned PX_BYTES = 64 * 4 * 4 * 3 * 2560, WS_BYTES = WS_PX + PX_BYTES, OOB = 0x80000000u;
constexpr int LDS_BYTES = BLOB + 10240;
constexpr float EPS = 1e-5f;
constexpr float QSCALE = 0.23570226039551584f * 1.4426950408889634f;
constexpr unsigned SPIN_CAP = 100000u, ONES = 0x3f803f80u;
constexpr int SC1 = 16;

DI unsigned rfl(unsigned x) { return __builtin_amdgcn_readfirstlane(x); }
DI srd_t mksrd(const void* p, unsigned bytes) { return __builtin_amdgcn_make_buffer_rsrc((void*)p, (short)0, (int)bytes, 0x00020000); }
DI void dma(unsigned ldsaddr, unsigned voff, srd_t srd, unsigned soff) {
    asm volatile("s_mov_b32 m0, %0\n\ts_nop 0\n\tbuffer_load_dwordx4 %1, %2, %3 offen lds" ::"s"(ldsaddr), "v"(voff), "s"(srd), "s"(soff) : "memory");
}
DI void dma_dev(unsigned ldsaddr, unsigned voff, srd_t srd, unsigned soff) {
    asm volatile("s_mov_b32 m0, %0\n\ts_nop 0\n\tbuffer_load_dwordx4 %1, %2, %3 offen sc1 lds" ::"s"(ldsaddr), "v"(voff), "s"(srd), "s"(soff) : "memory");
}
DI void touch(unsigned& sink, unsigned voff, srd_t srd, unsigned soff) {
    asm volatile("buffer_load_dword %0, %1, %2, %3 offen\n\ts_waitcnt vmcnt(3)" : "+v"(sink) : "v"(voff), "s"(srd), "s"(soff) : "memory");
}
DI s16x4 trrd(LAS const char* p) { return __builtin_amdgcn_ds_read_tr16_b64_v4i16((LAS s16x4*)p); }
DI bf16x8 rd128(LAS const char* p) { return *(LAS const bf16x8*)p; }
DI bf16x8 cat(s16x4 a, s16x4 b) { return __builtin_shufflevector(a, b, 0, 1, 2, 3, 4, 5, 6, 7); }
DI bf16x8 tr2(LAS const char* p, int second) { return cat(trrd(p), trrd(p + second)); }
typedef __bf16 bfx2 __attribute__((ext_vector_type(2)));
DI unsigned cvtpk(float lo, float hi) { return __builtin_bit_cast(unsigned, __builtin_convertvector((f32x2){lo, hi}, bfx2)); }
DI float blo(unsigned x) { return __uint_as_float(x << 16); }
DI float bhi(unsigned x) { return __uint_as_float(x & 0xffff0000u); }
DI float swapadd32(float v) { auto r = __builtin_amdgcn_permlane32_swap(__float_as_uint(v), __float_as_uint(v), false, false); return __uint_as_float(r[0]) + __uint_as_float(r[1]); }
DI float swapmax32(float v) { auto r = __builtin_amdgcn_permlane32_swap(__float_as_uint(v), __float_as_uint(v), false, false); return __builtin_fmaxf(__uint_as_float(r[0]), __uint_as_float(r[1])); }
DI float lowhalf(float v) { auto r = __builtin_amdgcn_permlane32_swap(__float_as_uint(v), __float_as_uint(v), false, false); return __uint_as_float(r[0]); }
DI float swapadd16(float v) { auto r = __builtin_amdgcn_permlane16_swap(__float_as_uint(v), __float_as_uint(v), false, false); return __uint_as_float(r[0]) + __uint_as_float(r[1]); }
DI f32x2 pfma(f32x2 a, f32x2 b, f32x2 c) { return __builtin_elementwise_fma(a, b, c); }
DI f32x2 splat(float a) { return (f32x2){a, a}; }
DI f32x2 lo2(f32x4 v) { return (f32x2){v[0], v[1]}; }
DI f32x2 hi2(f32x4 v) { return (f32x2){v[2], v[3]}; }
DI f32x2 unpk(unsigned x) { return (f32x2){blo(x), bhi(x)}; }
DI unsigned cvt2(f32x2 v) { return cvtpk(v[0], v[1]); }
DI void st128(u32x4 v, srd_t r, unsigned vo, unsigned so) { __builtin_amdgcn_raw_buffer_store_b128(v, r, vo, so, SC1); asm volatile("s_nop 1" ::"v"(v)); }
DI void st64(u32x2 v, srd_t r, unsigned vo, unsigned so) { __builtin_amdgcn_raw_buffer_store_b64(v, r, vo, so, SC1); }
DI void st32(unsigned v, srd_t r, unsigned vo, unsigned so) { __builtin_amdgcn_raw_buffer_store_b32(v, r, vo, so, SC1); }
DI u32x4 ld128(srd_t r, unsigned vo, unsigned so) { return __builtin_amdgcn_raw_buffer_load_b128(r, vo, so, SC1); }
DI u32x2 ld64(srd_t r, unsigned vo, unsigned so) { return __builtin_amdgcn_raw_buffer_load_b64(r, vo, so, SC1); }
DI unsigned ld32(srd_t r, unsigned vo, unsigned so) { return __builtin_amdgcn_raw_buffer_load_b32(r, vo, so, SC1); }
#define MFMA16(a, b, c) __builtin_amdgcn_mfma_f32_16x16x32_bf16(a, b, c, 0, 0, 0)
#define MFMA32(a, b, c) __builtin_amdgcn_mfma_f32_32x32x16_bf16(a, b, c, 0, 0, 0)
#define MFMA32K8(a, b, c) __builtin_amdgcn_mfma_f32_32x32x8bf16_1k(a, b, c, 0, 0, 0)
#define BAR() asm volatile("s_barrier" ::: "memory")
#define SB() __builtin_amdgcn_sched_barrier(0)
#define SGB(m, n) __builtin_amdgcn_sched_group_barrier(m, n, 0)
#define WAITL() do { SB(); __builtin_amdgcn_s_waitcnt(0xC07F); SB(); } while (0)
#define UNR _Pragma("unroll")
#define ROLL _Pragma("unroll 1")
template <int N> DI void waitv() { asm volatile("s_waitcnt vmcnt(%0)" ::"i"(N) : "memory"); }
#define Z8 ((bf16x8){0, 0, 0, 0, 0, 0, 0, 0})
template <int N> using IC = std::integral_constant<int, N>;
#define PROFILE(k)
#define STAMP(i)
#define PROFARG
#define PROFPASS

DI void raise(srd_t sw, unsigned off, int lane) { waitv<0>(); st32(1u, sw, lane == 0 ? 0u : OOB, off); }
DI void await4(srd_t sw, unsigned voff) {
    for (unsigned spins = 0; spins < SPIN_CAP; spins++) {
        const u32x4 v = ld128(sw, voff, 0);
        if (__all(voff == OOB || (v[0] & v[1] & v[2] & v[3]) == 1u)) break;
        __builtin_amdgcn_s_sleep(2);
    }
    asm volatile("" ::: "memory");
}
template <int NT, int NS, int PW, int PA, class IW, class IA> DI void ring_rest(IW iw, IA ia) {
    constexpr int P = PW + PA;
    UNR for (int t = 0; t < NS; t++) ia(t, t);
    waitv<(NS - 1) * PA>(); BAR();
    waitv<(NS - 2) * PA>(); BAR(); iw(NS, 0); ia(NS, 0);
    waitv<(NS - 3) * PA + P>(); BAR(); iw(NS + 1, 1); ia(NS + 1, 1);
    if constexpr (NS == 5) { waitv<PA + 2 * P>(); BAR(); iw(NS + 2, 2); ia(NS + 2, 2); }
    int slot = NS - 2;
    ROLL for (int t = NS - 1; t <= NT - NS; t++) { waitv<(NS - 2) * P>(); BAR(); iw(t + NS - 1, slot); ia(t + NS - 1, slot); slot = (slot == NS - 1) ? 0 : slot + 1; }
    if constexpr (NS == 5) { waitv<3 * P>(); BAR(); }
    waitv<2 * P>(); BAR(); waitv<P>(); BAR(); waitv<0>(); BAR();
}

constexpr int Q_STG = 32768, Q_NS = 4, Q_ST = Q_NS * Q_STG, Q_TAB = Q_ST + 1536;
template <bool VT> DI void qkv_mfma(char* lds, srd_t sw, int mt, int nt, int wid, int lane PROFARG) {
    const int wn = wid & 1, wsn = wid >> 1, r = lane & 31, h = lane >> 5, g = lane >> 4, q = (lane >> 2) & 3, p = lane & 3;
    unsigned xoff[2][2], woff[2];
    UNR for (int b = 0; b < 2; b++) { const int row = wsn * 64 + b * 32 + r; UNR for (int e = 0; e < 2; e++) xoff[b][e] = row * 128 + (((2 * e + h) ^ ((row >> 1) & 3)) | (((row >> 1) & 4))) * 16; }
    UNR for (int a = 0; a < 2; a++) woff[a] = 16384 + (8 * h + q) * 256 + ((((wn * 2 + a) * 2 + (g & 1)) ^ (q << 1)) * 32) + p * 8;
    f32x16 acc[2][2];
    UNR for (int a = 0; a < 2; a++) UNR for (int b = 0; b < 2; b++) acc[a][b] = (f32x16){};
    bf16x8 xw[2][2], xx[2][2], yw[2][2] = {{Z8, Z8}, {Z8, Z8}}, yx[2][2] = {{Z8, Z8}, {Z8, Z8}};
#define QRD(W, X, hf) do { UNR for (int e = 0; e < 2; e++) { W[e][0] = tr2(base + woff[0] + (2 * (hf) + e) * 4096, 1024); X[e][0] = rd128(base + (xoff[0][e] ^ ((hf) * 64))); \
        X[e][1] = rd128(base + (xoff[1][e] ^ ((hf) * 64))); W[e][1] = tr2(base + woff[1] + (2 * (hf) + e) * 4096, 1024); } } while (0)
#define QMM(W, X) do { UNR for (int e = 0; e < 2; e++) UNR for (int a = 0; a < 2; a++) UNR for (int b = 0; b < 2; b++) acc[a][b] = VT ? MFMA32(X[e][b], W[e][a], acc[a][b]) : MFMA32(W[e][a], X[e][b], acc[a][b]); } while (0)
#define QSG() do { UNR for (int i = 0; i < 4; i++) { SGB(0x8, 1); SGB(0x100, 3); } SGB(0x8, 8); } while (0)
    int slot = 0;
    ROLL for (int t = 0; t < 9; t++) {
        BAR(); if (t == 0) STAMP(0); LAS const char* base = (LAS const char*)lds + slot * Q_STG;
        SB(); QRD(xw, xx, 0); QMM(yw, yx); QSG(); WAITL(); QRD(yw, yx, 1); QMM(xw, xx); QSG(); WAITL();
        slot = (slot == Q_NS - 1) ? 0 : slot + 1;
    }
    QMM(yw, yx); STAMP(1);
    BAR();
    LAS const float* mu = (LAS const float*)(lds + Q_ST); LAS const float* rs = mu + 128; LAS const float* cs = mu + 256;
    if constexpr (!VT) {
        const float sc = nt * 128 + wn * 64 >= D ? 1.f : QSCALE;
        f32x2 m_[2], r_[2];
        UNR for (int b = 0; b < 2; b++) { m_[b] = splat(-mu[wsn * 64 + b * 32 + r]); r_[b] = splat(rs[wsn * 64 + b * 32 + r] * sc); }
        LAS char* img = (LAS char*)lds + (wsn * 64 + wn * 16 + h) * 256 + r * 8;
        UNR for (int a = 0; a < 2; a++) UNR for (int m = 0; m < 4; m++) {
            const f32x4 c4 = *(LAS const f32x4*)(cs + wn * 64 + a * 32 + 8 * m + 4 * h);
            UNR for (int b = 0; b < 2; b++)
                *(LAS u32x2*)(img + (b * 32 + 8 * a + 2 * m) * 256) = (u32x2){cvt2(pfma(m_[b], lo2(c4), (f32x2){acc[a][b][4 * m], acc[a][b][4 * m + 1]}) * r_[b]),
                                                                             cvt2(pfma(m_[b], hi2(c4), (f32x2){acc[a][b][4 * m + 2], acc[a][b][4 * m + 3]}) * r_[b])};
        }
    } else {
        unsigned vo[2]; f32x2 c1[2];
        UNR for (int a = 0; a < 2; a++) {
            const int nl = wn * 64 + a * 32 + r, col = nt * 128 + nl - 2 * D, head = (col * 3641) >> 16;
            vo[a] = col < D ? head * (BLOB - 18 * 16) + col * 16 + h * 304 : OOB; c1[a] = splat(-cs[nl]);
        }
        UNR for (int b = 0; b < 2; b++) {
            const int T = mt * 4 + wsn * 2 + b; const unsigned so = WS_KV + (T >> 3) * CHUNK + 9216 + (T & 7) * 1216;
            UNR for (int s2 = 0; s2 < 2; s2++) {
                f32x4 m4[2], r4[2];
                UNR for (int e2 = 0; e2 < 2; e2++) { const int tl = wsn * 64 + b * 32 + 16 * s2 + 8 * e2 + 4 * h; m4[e2] = *(LAS const f32x4*)(mu + tl); r4[e2] = *(LAS const f32x4*)(rs + tl); }
                UNR for (int a = 0; a < 2; a++) {
                    u32x4 o;
                    UNR for (int e2 = 0; e2 < 2; e2++) { const int i = 8 * s2 + 4 * e2;
                        o[2 * e2] = cvt2(pfma(lo2(m4[e2]), c1[a], (f32x2){acc[a][b][i], acc[a][b][i + 1]}) * lo2(r4[e2]));
                        o[2 * e2 + 1] = cvt2(pfma(hi2(m4[e2]), c1[a], (f32x2){acc[a][b][i + 2], acc[a][b][i + 3]}) * hi2(r4[e2])); }
                    st128(o, sw, vo[a], so + s2 * 608);
                }
            }
        }
    }
#undef QRD
#undef QMM
#undef QSG
}
DI void phase_qkv(char* lds, const bf16_t* x, const bf16_t* w, srd_t sw, int bid, int tid, int wid, int lane PROFARG) {
    if (bid >= 224) {
        const unsigned i = (bid - 224) * 768 + tid;
        st128((u32x4){~0u, ~0u, ~0u, ~0u}, sw, i < (WS_F2 - WS_ST) / 16 ? i * 16 : OOB, WS_ST);
        st128((u32x4){0u, 0u, 0u, 0u}, sw, i < (WS_F1 - WS_F2) / 16 ? i * 16 : OOB, WS_F2);
        st128((u32x4){ONES, ONES, ONES, ONES}, sw, i < 8192 ? (i >> 8) * BLOB + ((i >> 5) & 7) * CHUNK + (i & 31) * 304 : OOB, WS_KV + 9216 + 288);
        unsigned sink = 0; const int xg = bid & 7, j = ((bid - 224) >> 3) * 12 + wid;
        const srd_t sx = mksrd(x, S * D * 2), sq = mksrd(w, D * 3 * D * 2);
        { const int l = j * 64 + lane, row = l / 6; touch(sink, ((xg >> 1) * 512 + row) * 1152 + (3 + l - 6 * row) * 128, sx, 0); }
        UNR for (int k = 0; k < 2; k++) { const int l = (j + 48 * k) * 64 + lane, row = l / 14; touch(sink, row < 384 ? (192 + row) * 3456 + (xg & 1) * 1792 + (l - 14 * row) * 128 : OOB, sq, 0); }
        asm volatile("s_waitcnt vmcnt(0)" : "+v"(sink)::"memory");
        return;
    }
    const int xg = bid & 7, idx = bid >> 3, mt = (xg >> 1) * 4 + (idx & 3), nt = (xg & 1) * 7 + (idx >> 2);
    const unsigned lds0 = (unsigned)(uintptr_t)lds;
    if (wid < 4) { if (nt >= 9) qkv_mfma<true>(lds, sw, mt, nt, wid, lane PROFPASS); else qkv_mfma<false>(lds, sw, mt, nt, wid, lane PROFPASS); }
    else if (wid < 8) {
        const int lw = wid - 4; const srd_t sx = mksrd(x, S * D * 2), sq = mksrd(w, D * 3 * D * 2);
        unsigned vx[2];
        UNR for (int e = 0; e < 2; e++) vx[e] = (mt * 128 + 32 * lw + (lane >> 3)) * 1152 + ((lane & 7) ^ (4 * e + (lane >> 4))) * 16;
        const int rq = lane >> 4, c = lane & 15; const unsigned vw = (16 * lw + rq) * 3456 + nt * 256 + ((((c >> 1) ^ (rq << 1)) * 2) + (c & 1)) * 16;
        auto issue = [&](int t, int slot) {
            const unsigned lb = lds0 + slot * Q_STG;
            UNR for (int i = 0; i < 4; i++) dma(lb + 16384 + (16 * lw + 4 * i) * 256, vw, sq, (t * 64 + 4 * i) * 3456);
            UNR for (int i = 0; i < 4; i++) dma(lb + (32 * lw + 8 * i) * 128, vx[i & 1], sx, 8 * i * 1152 + t * 128);
        };
        issue(0, 0); issue(1, 1); waitv<8>(); BAR(); issue(2, 2); issue(3, 3);
        int slot = 0;
        ROLL for (int t = 1; t <= 5; t++) { waitv<16>(); BAR(); issue(t + 3, slot); slot = (slot + 1) & 3; }
        waitv<16>(); BAR(); waitv<8>(); BAR(); waitv<0>(); BAR();
        BAR();
    } else {
        const int tw = wid - 8, r = lane & 31, h = lane >> 5, g = lane >> 4, q = (lane >> 2) & 3, p = lane & 3, row = tw * 32 + r;
        const unsigned woff = 16384 + (8 * h + q) * 256 + (((tw * 2 + (g & 1)) ^ (q << 1)) * 32) + p * 8;
        if (tw < 2) {
            const int T = mt * 4 + tw * 2 + h, n = nt * 128 + r * 4; const bool isk = n >= D;
            const int Qd = (n - (isk ? D : 0)) >> 2, ph = (Qd * 57) >> 9, qm = Qd - 9 * ph, head = 2 * ph + (qm > 4), pq = qm > 4 ? qm - 5 : qm;
            const unsigned kb = WS_KV + head * BLOB + (T >> 3) * CHUNK;
            const unsigned em = (isk ? kb + (T & 7) * 1024 : WS_QB + (head * 64 + T) * 1024) + (pq & 1) * 512 + (pq >> 1) * 256, er = isk ? kb + 8192 + (T & 7) * 128 : WS_QR + (head * 64 + T) * 128;
            *((LAS u32x2*)(lds + Q_TAB) + tw * 64 + lane) = (u32x2){qm == 4 ? er : em, qm == 4 ? (isk ? BLOB : 8192) : 0u};
        }
        float a1[4] = {0.f, 0.f, 0.f, 0.f}, a2[4] = {0.f, 0.f, 0.f, 0.f}; f32x4 ac = {0.f, 0.f, 0.f, 0.f}; int slot = 0;
        const unsigned one = (lane & 15) == (g & 1) ? ONES : 0u; const bf16x8 sel = __builtin_bit_cast(bf16x8, (u32x4){one, one, one, one});
        ROLL for (int t = 0; t < 9; t++) {
            BAR(); LAS const char* base = (LAS const char*)lds + slot * Q_STG;
            u32x4 xv[4]; bf16x8 wv[4];
            UNR for (int ks = 0; ks < 4; ks++) xv[ks] = *(LAS const u32x4*)(base + row * 128 + (((2 * ks + h) ^ ((row >> 1) & 7)) * 16));
            UNR for (int ks = 0; ks < 4; ks++) wv[ks] = tr2(base + woff + ks * 4096, 1024);
            WAITL();
            UNR for (int ks = 0; ks < 4; ks++) {
                ac = MFMA16(sel, wv[ks], ac);
                UNR for (int e = 0; e < 4; e += 2) {
                    const float v0 = blo(xv[ks][e]), v1 = bhi(xv[ks][e]), v2 = blo(xv[ks][e + 1]), v3 = bhi(xv[ks][e + 1]);
                    asm("v_add_f32 %0, %0, %8\n\tv_add_f32 %1, %1, %9\n\tv_add_f32 %2, %2, %10\n\tv_add_f32 %3, %3, %11\n\tv_fmac_f32 %4, %8, %8\n\tv_fmac_f32 %5, %9, %9\n\tv_fmac_f32 %6, %10, %10\n\tv_fmac_f32 %7, %11, %11"
                        : "+v"(a1[0]), "+v"(a1[1]), "+v"(a1[2]), "+v"(a1[3]), "+v"(a2[0]), "+v"(a2[1]), "+v"(a2[2]), "+v"(a2[3]) : "v"(v0), "v"(v1), "v"(v2), "v"(v3));
                }
            }
            slot = (slot + 1) & 3;
        }
        const float s1 = swapadd32((a1[0] + a1[1]) + (a1[2] + a1[3])), s2 = swapadd32((a2[0] + a2[1]) + (a2[2] + a2[3]));
        const float m = s1 * (1.f / D), var = __builtin_fmaxf(__builtin_fmaf(-m, m, s2 * (1.f / D)), 0.f);
        LAS float* st = (LAS float*)(lds + Q_ST);
        if (h == 0) { st[row] = m; st[128 + row] = __builtin_amdgcn_rsqf(var + EPS); }
        if (lane < 16) { st[256 + tw * 32 + lane] = ac[0]; st[256 + tw * 32 + 16 + lane] = ac[1]; }
        WAITL(); BAR();
    }
    if (nt < 9) {
        WAITL(); BAR(); STAMP(2);
        UNR for (int k = 0; k < 3; k++) {
            const int c = tid + 768 * k, c16 = c & 15;
            if (k < 2 || wid < 8) {
                const u32x2 e = *((LAS const u32x2*)(lds + Q_TAB) + (c >> 4));
                LAS const char* a = (LAS const char*)lds + (c >> 4) * 256 + (e[1] ? (c16 & 7) * 32 : c16 * 16);
                u32x4 v = *(LAS const u32x4*)a; unsigned dst = e[0] + c16 * 16;
                if (__builtin_amdgcn_ballot_w64(e[1] != 0)) {
                    const u32x4 b = *(LAS const u32x4*)(a + 16);
                    if (e[1]) { v = c16 < 8 ? (u32x4){v[0], v[2], b[0], b[2]} : (u32x4){v[1], v[3], b[1], b[3]}; dst = c16 < 8 ? dst : dst + e[1] - 128; }
                }
                st128(v, sw, dst, 0);
            }
        }
    }
}

#define AT_IT(SC, SN, c0, c1, c2, c3, c4, c5, c6, c7, c8, c9, c10, c11, c12, c13, c14, c15, KO0, KO1, KR, V1, V0) \
    "s_waitcnt lgkmcnt(2)\n v_mfma_f32_32x32x16_bf16 " SN ", v[56:59], v[70:73], 0\n ds_read2st64_b64 v[56:59], v76 offset0:" KO0 " offset1:" KO1 "\n" \
    "v_exp_f32 " c0 "," c0 "\n v_exp_f32 " c1 "," c1 "\n v_exp_f32 " c2 "," c2 "\n v_exp_f32 " c3 "," c3 "\n v_cvt_pk_bf16_f32 v48," c0 "," c1 "\n v_cvt_pk_bf16_f32 v49," c2 "," c3 "\n" \
    "v_mfma_f32_32x32x8_bf16 " SN ", v[60:61], v[74:75], " SN "\n ds_read_b32 v60, v77 offset:" KR "\n" \
    "v_exp_f32 " c4 "," c4 "\n v_exp_f32 " c5 "," c5 "\n v_exp_f32 " c6 "," c6 "\n v_exp_f32 " c7 "," c7 "\n v_cvt_pk_bf16_f32 v50," c4 "," c5 "\n v_cvt_pk_bf16_f32 v51," c6 "," c7 "\n" \
    "s_waitcnt lgkmcnt(2)\n v_mfma_f32_32x32x16_bf16 v[32:47], v[66:69], v[52:55], v[32:47]\n" \
    "v_exp_f32 " c8 "," c8 "\n v_exp_f32 " c9 "," c9 "\n v_exp_f32 " c10 "," c10 "\n v_exp_f32 " c11 "," c11 "\n v_cvt_pk_bf16_f32 v52," c8 "," c9 "\n v_cvt_pk_bf16_f32 v53," c10 "," c11 "\n" \
    "v_mfma_f32_32x32x16_bf16 v[32:47], v[62:65], v[48:51], v[32:47]\n" \
    "s_mov_b64 exec, s[36:37]\n ds_read_b128 v[66:69], v78 offset:" V1 "\n ds_read_b128 v[62:65], v78 offset:" V0 "\n s_mov_b64 exec, -1\n" \
    "v_exp_f32 " c12 "," c12 "\n v_exp_f32 " c13 "," c13 "\n v_exp_f32 " c14 "," c14 "\n v_exp_f32 " c15 "," c15 "\n v_cvt_pk_bf16_f32 v54," c12 "," c13 "\n v_cvt_pk_bf16_f32 v55," c14 "," c15 "\n"
#define AT_A(KO0, KO1, KR, V1, V0) AT_IT("v[0:15]", "v[16:31]", "v0", "v1", "v2", "v3", "v4", "v5", "v6", "v7", "v8", "v9", "v10", "v11", "v12", "v13", "v14", "v15", KO0, KO1, KR, V1, V0)
#define AT_B(KO0, KO1, KR, V1, V0) AT_IT("v[16:31]", "v[0:15]", "v16", "v17", "v18", "v19", "v20", "v21", "v22", "v23", "v24", "v25", "v26", "v27", "v28", "v29", "v30", "v31", KO0, KO1, KR, V1, V0)
#define AT_SWAP 6

DI void attn_loader(char* lds, char* ws, const bf16_t* wo, const bf16_t* w1, const bf16_t* w2, srd_t sw, unsigned long long tag, int bid, int lane PROFARG) {
    const int head = (bid & 7) * 4 + ((bid >> 3) & 3); const unsigned lds0 = (unsigned)(uintptr_t)lds, v16 = lane * 16;
    const srd_t skv = mksrd(ws + WS_KV + (size_t)head * BLOB, BLOB);
    const unsigned tl = (unsigned)tag, th = (unsigned)(tag >> 32);
    for (unsigned spins = 0; spins < SPIN_CAP; spins++) {
        const u32x4 a = ld128(sw, lane * 32, WS_F1), b = ld128(sw, lane * 32 + 16, WS_F1);
        if (__all(a[0] == tl && a[1] == th && a[2] == tl && a[3] == th && b[0] == tl && b[1] == th && b[2] == tl && b[3] == th)) break;
        __builtin_amdgcn_s_sleep(2);
    }
    STAMP(3);
    BAR();
    ROLL for (int pc = 19; pc < 36; pc++) dma_dev(lds0 + pc * 1024, v16, skv, pc * 1024);
    BAR();
    ROLL for (int pc = 36; pc < 148; pc += 4) { waitv<40>(); UNR for (int i = 0; i < 4; i++) dma_dev(lds0 + (pc + i) * 1024, v16, skv, (pc + i) * 1024); }
    waitv<0>(); BAR();
    unsigned sink = 0; const int xg = bid & 7, i = bid >> 3; const srd_t s0 = mksrd(wo, D * D * 2), s1 = mksrd(w1, S * F * 2), s2 = mksrd(w2, S * F * 2);
    ROLL for (int k = 0; k < 9; k++) touch(sink, ((xg >> 1) * 1152 + 36 * i + (lane >> 4)) * (S * 2) + (xg & 1) * 2048 + (lane & 15) * 128, s2, k * 4 * (S * 2));
    ROLL for (int k = 0; k < 10; k++) touch(sink, (64 * i + lane) * (F * 2) + ((9 * xg) >> 1) * 256, s1, k * 128);
    ROLL for (int k = 0; k < 3; k++) touch(sink, (i * 162 + lane) * 128, s0, k * 8192);
    asm volatile("s_waitcnt vmcnt(0)" : "+v"(sink)::"memory");
}
DI void attn_compute(char* lds, char* ws, srd_t sw, int bid, int wid, int lane PROFARG) {
    const int head = (bid & 7) * 4 + ((bid >> 3) & 3), T = (bid >> 5) * 8 + wid, r = lane & 31, h = lane >> 5;
    const unsigned lds0 = (unsigned)(uintptr_t)lds, v16 = lane * 16;
    const srd_t skv = mksrd(ws + WS_KV + (size_t)head * BLOB, BLOB);
    const unsigned ka = h * 256 + r * 8, kra = 8192 + r * 4, va = 9216 + h * 304 + r * 16;
    BAR(); STAMP(3);
    dma_dev(lds0 + wid * 1024, v16, skv, wid * 1024); dma_dev(lds0 + (wid + 8) * 1024, v16, skv, (wid + 8) * 1024);
    if (wid < 3) dma_dev(lds0 + (wid + 16) * 1024, v16, skv, (wid + 16) * 1024);
    const u32x2 q0 = ld64(sw, ka, WS_QB + (head * 64 + T) * 1024), q1 = ld64(sw, ka + 512, WS_QB + (head * 64 + T) * 1024);
    u32x2 qr = {ld32(sw, h ? OOB : r * 4, WS_QR + (head * 64 + T) * 128), 0u};
    const bf16x8 qm = __builtin_bit_cast(bf16x8, (u32x4){q0[0], q0[1], q1[0], q1[1]});
    waitv<0>(); BAR(); STAMP(4);
    {
        LAS const char* b = (LAS const char*)lds;
        const u32x2 k0 = *(LAS const u32x2*)(b + ka), k1 = *(LAS const u32x2*)(b + ka + 512); const u32x2 kr = {*(LAS const unsigned*)(b + kra), 0u};
        f32x16 s = MFMA32(__builtin_bit_cast(bf16x8, (u32x4){k0[0], k0[1], k1[0], k1[1]}), qm, (f32x16){});
        s = MFMA32K8(__builtin_bit_cast(s16x4, kr), __builtin_bit_cast(s16x4, qr), s);
        float m = s[0]; UNR for (int i = 1; i < 16; i++) m = __builtin_fmaxf(m, s[i]);
        m = swapmax32(m);
        qr[1] = h ? 0u : (cvtpk(-m, 0.f) & 0xffffu);
    }
    f32x16 O;
    {
        unsigned a0 = lds0 + ka, a1 = lds0 + kra, a2 = lds0 + va;
        asm volatile(
            "s_mov_b32 s36, 0x7ffff\n s_mov_b32 s37, 0x7ffff\n s_mov_b32 s38, 0\n"
            "s_setprio 1\n s_cmp_ge_u32 %[wid], 4\n s_cbranch_scc1 1f\n s_setprio 2\n 1:\n"
            "ds_read2st64_b64 v[56:59], v76 offset0:0 offset1:1\n ds_read_b32 v60, v77\n"
            "v_mov_b32 v61, 0x3f80\n"
            "v_mov_b32 v32, 0\n v_mov_b32 v33, 0\n v_mov_b32 v34, 0\n v_mov_b32 v35, 0\n v_mov_b32 v36, 0\n v_mov_b32 v37, 0\n v_mov_b32 v38, 0\n v_mov_b32 v39, 0\n"
            "v_mov_b32 v40, 0\n v_mov_b32 v41, 0\n v_mov_b32 v42, 0\n v_mov_b32 v43, 0\n v_mov_b32 v44, 0\n v_mov_b32 v45, 0\n v_mov_b32 v46, 0\n v_mov_b32 v47, 0\n"
            "v_mov_b32 v52, 0\n v_mov_b32 v53, 0\n v_mov_b32 v54, 0\n v_mov_b32 v55, 0\n"
            "v_mov_b32 v62, 0\n v_mov_b32 v63, 0\n v_mov_b32 v64, 0\n v_mov_b32 v65, 0\n v_mov_b32 v66, 0\n v_mov_b32 v67, 0\n v_mov_b32 v68, 0\n v_mov_b32 v69, 0\n"
            "s_waitcnt lgkmcnt(0)\n"
            "v_mfma_f32_32x32x16_bf16 v[0:15], v[56:59], v[70:73], 0\n v_mfma_f32_32x32x8_bf16 v[0:15], v[60:61], v[74:75], v[0:15]\n"
            "ds_read2st64_b64 v[56:59], v76 offset0:2 offset1:3\n ds_read_b32 v60, v77 offset:128\n"
            "s_mov_b64 exec, s[36:37]\n ds_read_b128 v[66:69], v78 offset:608\n ds_read_b128 v[62:65], v78\n s_mov_b64 exec, -1\n"
            "s_nop 7\n s_nop 7\n"
            "2:\n"
            AT_A("4", "5", "256", "608", "1216") AT_B("6", "7", "384", "1824", "2432") AT_A("8", "9", "512", "3040", "3648") AT_B("10", "11", "640", "4256", "4864")
            AT_A("12", "13", "768", "5472", "6080") AT_B("14", "15", "896", "6688", "7296")
            "s_cmp_lg_u32 s38, 0\n s_cbranch_scc1 3f\n s_barrier\n 3:\n"
            AT_A("37", "38", "18944", "7904", "8512") AT_B("39", "40", "19072", "9120", "18944")
            "v_add_u32 v76, 0x4a00, v76\n v_add_u32 v77, 0x4a00, v77\n v_add_u32 v78, 0x4a00, v78\n"
            "s_add_u32 s38, s38, 1\n s_cmp_lg_u32 s38, %[swap]\n s_cbranch_scc1 4f\n s_cmp_ge_u32 %[wid], 4\n s_cbranch_scc1 4f\n s_setprio 0\n 4:\n"
            "s_cmp_lt_u32 s38, 8\n s_cbranch_scc1 2b\n"
            "s_waitcnt lgkmcnt(0)\n v_mfma_f32_32x32x16_bf16 v[32:47], v[66:69], v[52:55], v[32:47]\n s_setprio 0\n s_nop 7\n s_nop 7\n"
            : "={v[32:47]}"(O), "+{v76}"(a0), "+{v77}"(a1), "+{v78}"(a2)
            : "{v[70:73]}"(qm), "{v[74:75]}"(qr), [wid] "s"(wid), [swap] "i"(AT_SWAP)
            : "memory", "scc", "s36", "s37", "s38", "v0", "v1", "v2", "v3", "v4", "v5", "v6", "v7", "v8", "v9", "v10", "v11", "v12", "v13", "v14", "v15", "v16", "v17", "v18", "v19", "v20", "v21", "v22", "v23",
              "v24", "v25", "v26", "v27", "v28", "v29", "v30", "v31", "v48", "v49", "v50", "v51", "v52", "v53", "v54", "v55", "v56", "v57", "v58", "v59", "v60", "v61", "v62", "v63", "v64", "v65", "v66", "v67", "v68", "v69");
    }

    STAMP(5);
    float l = lowhalf(O[10]);
    const bool bad = !(l < 1.8e19f && l > 1e-18f);
    if (__builtin_expect(__any(bad), 0)) {
        LAS const char* b = (LAS const char*)lds; qr[1] = 0u; float m = -INFINITY;
        const unsigned vs = 9216 + h * 304 + (r < 19 ? r : r - 16) * 16;
        for (int pass = 0; pass < 2; pass++) {
            O = (f32x16){};
            for (int t = 0; t < 64; t++) {
                LAS const char* cb = b + (t >> 3) * CHUNK; const int i = t & 7;
                const u32x2 k0 = *(LAS const u32x2*)(cb + i * 1024 + ka), k1 = *(LAS const u32x2*)(cb + i * 1024 + ka + 512); const u32x2 kr = {*(LAS const unsigned*)(cb + i * 128 + kra), 0u};
                f32x16 s = MFMA32(__builtin_bit_cast(bf16x8, (u32x4){k0[0], k0[1], k1[0], k1[1]}), qm, (f32x16){});
                s = MFMA32K8(__builtin_bit_cast(s16x4, kr), __builtin_bit_cast(s16x4, qr), s);
                if (pass == 0) { UNR for (int e = 0; e < 16; e++) m = __builtin_fmaxf(m, s[e]); }
                else {
                    u32x4 p0, p1;
                    UNR for (int e = 0; e < 4; e++) { p0[e] = cvtpk(__builtin_amdgcn_exp2f(s[2 * e] - m), __builtin_amdgcn_exp2f(s[2 * e + 1] - m)); p1[e] = cvtpk(__builtin_amdgcn_exp2f(s[8 + 2 * e] - m), __builtin_amdgcn_exp2f(s[9 + 2 * e] - m)); }
                    asm volatile("s_nop 1" : "+v"(p0), "+v"(p1));
                    O = MFMA32(rd128(cb + i * 1216 + vs), __builtin_bit_cast(bf16x8, p0), O); O = MFMA32(rd128(cb + i * 1216 + 608 + vs), __builtin_bit_cast(bf16x8, p1), O);
                }
            }
            m = swapmax32(m);
        }
        l = lowhalf(O[10]);
    }

    BAR();
    const float il = __builtin_amdgcn_rcpf(l); const unsigned so = WS_OF + T * 36864, vo = (r >> 4) * 18432 + (r & 15) * 16;
    st128((u32x4){cvtpk(O[0] * il, O[1] * il), cvtpk(O[2] * il, O[3] * il), cvtpk(O[4] * il, O[5] * il), cvtpk(O[6] * il, O[7] * il)}, sw, vo + h * 256, so + (head >> 1) * 1024 + (head & 1) * 512);
    st32(cvtpk(O[8] * il, O[9] * il), sw, h ? OOB : vo, so + (16 + (head >> 4)) * 1024 + ((head & 15) >> 2) * 256 + (head & 3) * 4);
}

constexpr int O_X = 9 * 12288, O_T = O_X + 12288;
DI void wo_loader(char* lds, const bf16_t* x, const bf16_t* wo, srd_t sw, unsigned flag, int bid, int lw, int lane) {
    const int idx = bid >> 3, st = (bid & 7) * 4 + idx / 6, nt = idx % 6; const unsigned lds0 = (unsigned)(uintptr_t)lds;
    const srd_t so = mksrd(wo, D * D * 2), sx = mksrd(x, S * D * 2);
    unsigned vm[3], vr[3], vx[3];
    UNR for (int i = 0; i < 3; i++) {
        const int c = i * 64 + lane, rl = (c * 171) >> 11, cc = c - 12 * rl, g = rl >> 2;
        const int col = nt * 192 + (cc ^ ((g & 1) * 2)) * 16;
        vm[i] = (36 * (lw >> 1) + 8 * (lw & 1) + 18 * (g >> 1) + 4 * (g & 1) + (rl & 3)) * 1152 + col;
        vr[i] = (18 * (16 * (lw >> 1) + 2 * (lw & 1) + 4 * g + ((rl & 3) >> 1)) + 16 + (rl & 1)) * 1152 + col;
        vx[i] = (st * 64 + 16 * lw + rl) * 1152 + nt * 192 + cc * 16;
    }
    UNR for (int i = 0; i < 3; i++) dma(lds0 + O_X + (3 * lw + i) * 1024, vx[i], sx, 0);
    UNR for (int t = 0; t < 9; t++) {
        UNR for (int i = 0; i < 3; i++) dma(lds0 + t * 12288 + (3 * lw + i) * 1024, t < 8 ? vm[i] : vr[i], so, t < 8 ? t * 72 * 1152 : 0);
        if (t == 1) { waitv<9>(); st32(1u, sw, lane == 0 ? 0u : OOB, flag); }
    }
    waitv<25>(); BAR(); waitv<22>(); BAR();
    waitv<18>(); BAR(); waitv<15>(); BAR(); waitv<12>(); BAR(); waitv<9>(); BAR(); waitv<6>(); BAR(); waitv<3>(); BAR(); waitv<0>(); BAR();
    BAR();
}
DI void wo_mfma(char* lds, char* ws, srd_t sw, int bid, int wid, int lane PROFARG) {
    const int idx = bid >> 3, st = (bid & 7) * 4 + idx / 6, nt = idx % 6;
    const int s = lane & 15, g = lane >> 4, q = (lane >> 2) & 3, p = lane & 3, s16 = st * 4 + wid;
    const unsigned be = (4 * g + q) * 192 + (g & 1) * 32 + 8 * p, bo = (4 * g + q) * 192 - (g & 1) * 32 + 8 * p;
    unsigned ho[6];
    UNR for (int n = 0; n < 6; n++) { const int t16 = nt * 6 + n, dT = t16 / 9, tl = t16 - 9 * dT;
        ho[n] = dT * 4608 + (tl < 8 ? (tl >> 1) * 1024 + ((2 * (tl & 1) + (g >> 1)) * 16 + s) * 16 + (g & 1) * 8 : 4096 + lane * 8); }
    await4(sw, lane < 8 ? WS_F2 + (s16 >> 1) * 128 + lane * 16 : OOB);
    STAMP(6);
    bf16x8 of[18];
    UNR for (int b = 0; b < 18; b++) of[b] = __builtin_bit_cast(bf16x8, ld128(sw, lane * 16 + (b & 3) * 1024, WS_OF + s16 * 18432 + (b >> 2) * 4096));
    f32x4 acc[6]; u32x2 xv[6];
    UNR for (int t = 0; t < 6; t++) acc[t] = (f32x4){0.f, 0.f, 0.f, 0.f};
    UNR for (int t = 0; t < 9; t++) {
        BAR(); LAS const char* base = (LAS const char*)lds + t * 12288;
        if (t == 0) UNR for (int n = 0; n < 6; n++) xv[n] = *(LAS const u32x2*)((LAS const char*)lds + O_X + (wid * 16 + s) * 192 + n * 32 + g * 8);
        bf16x8 af[12];
        UNR for (int kb = 0; kb < 2; kb++) UNR for (int n = 0; n < 6; n++) af[kb * 6 + n] = tr2(base + kb * 6144 + ((n & 1) ? bo : be) + n * 32, 3072);
        SB();
        UNR for (int kb = 0; kb < 2; kb++) UNR for (int n = 0; n < 6; n++) acc[n] = MFMA16(af[kb * 6 + n], of[2 * t + kb], acc[n]);
    }
    WAITL(); BAR(); STAMP(7);
    f32x2 hv[6][2], a1 = {0.f, 0.f}, a2 = {0.f, 0.f};
    UNR for (int n = 0; n < 6; n++) {
        hv[n][0] = lo2(acc[n]) + unpk(xv[n][0]); hv[n][1] = hi2(acc[n]) + unpk(xv[n][1]);
        UNR for (int e = 0; e < 2; e++) { a1 += hv[n][e]; a2 = pfma(hv[n][e], hv[n][e], a2); }
    }
    const float s1 = swapadd32(swapadd16(a1[0] + a1[1])), s2 = swapadd32(swapadd16(a2[0] + a2[1]));
    const unsigned gs = WS_ST + (s16 * 16 + s) * 48;
    { unsigned b2 = __float_as_uint(s2); if (b2 == ~0u) b2 = 0x7fc00000u; st64((u32x2){__float_as_uint(s1), b2}, sw, g == 0 ? gs + nt * 8 : OOB, 0); }
    UNR for (int n = 0; n < 6; n++) st64((u32x2){cvt2(hv[n][0]), cvt2(hv[n][1])}, sw, ho[n], WS_HF + s16 * 18432);
    u32x2 w0, w1;
    for (unsigned spins = 0; spins < SPIN_CAP; spins++) {
        w0 = ld64(sw, gs + g * 8, 0); w1 = ld64(sw, g < 2 ? gs + g * 8 + 32 : OOB, 0);
        if (__all(w0[1] != ~0u && w1[1] != ~0u)) break;
        __builtin_amdgcn_s_sleep(1);
    }
    STAMP(8);
    const float t1 = swapadd32(swapadd16(__uint_as_float(w0[0]) + __uint_as_float(w1[0]))), t2 = swapadd32(swapadd16(__uint_as_float(w0[1]) + __uint_as_float(w1[1])));
    const float m = t1 * (1.f / D), var = __builtin_fmaxf(__builtin_fmaf(-m, m, t2 * (1.f / D)), 0.f);
    const float r1 = __builtin_amdgcn_rsqf(var + EPS), rr = r1 * __builtin_amdgcn_rsqf(var * r1 * r1 + EPS);
    LAS char* tb = (LAS char*)lds + O_T + wid * 3328;
    const f32x2 r2 = splat(rr), mr = splat(-m * rr);
    UNR for (int n = 0; n < 6; n++) *(LAS u32x2*)(tb + s * 208 + n * 32 + g * 8) = (u32x2){cvt2(pfma(hv[n][0], r2, mr)), cvt2(pfma(hv[n][1], r2, mr))};
    WAITL();
    UNR for (int dt = 0; dt < 3; dt++)
        st128(__builtin_bit_cast(u32x4, tr2(tb + (4 * (g >> 1) + q) * 208 + dt * 64 + (g & 1) * 32 + p * 8, 8 * 208)), sw, lane * 16, WS_HH + ((nt * 128 + s16) * 3 + dt) * 1024);
    STAMP(22); raise(sw, WS_F3 + (nt * 128 + s16) * 4, lane); STAMP(23);
}

constexpr int A_NS = 5, A_H = A_NS * 16384;
DI void up_loader(char* lds, char* ws, const bf16_t* w1, srd_t sw, int bid, int lw, int lane PROFARG) {
    const int L = (bid & 7) * 27 + (bid >> 3), ft = L / 6, dT = L % 6, rq = lane >> 4, c = lane & 15; const unsigned lds0 = (unsigned)(uintptr_t)lds;
    const srd_t s1 = mksrd(w1, S * F * 2), sh = mksrd(ws + WS_HH, S * D * 2);
    const unsigned vw = (16 * lw + rq) * (F * 2) + ft * 256 + ((((c >> 1) ^ (rq << 1)) * 2) + (c & 1)) * 16, vh = lane * 16;
    auto iw = [&](int t, int slot) { UNR for (int i = 0; i < 4; i++) dma(lds0 + slot * 16384 + (16 * lw + 4 * i) * 256, vw, s1, (t * 64 + 4 * i) * (F * 2)); };
    auto ia = [&](int t, int slot) { UNR for (int j = 0; j < 3; j++) dma_dev(lds0 + A_H + slot * 12288 + (lw * 3 + j) * 1024, vh, sh, (dT * 32 + t) * 12288 + (lw * 3 + j) * 1024); };
    UNR for (int t = 0; t < A_NS; t++) iw(t, t);
    if (lw == 0) await4(sw, lane < 32 ? WS_F3 + dT * 512 + lane * 16 : OOB);
    BAR(); STAMP(9);
    ring_rest<32, A_NS, 4, 3>(iw, ia);
    BAR(); BAR();
}
DI void up_mfma(char* lds, char* ws, const bf16_t* b1, srd_t sw, int bid, int wid, int lane PROFARG) {
    const int L = (bid & 7) * 27 + (bid >> 3), ft = L / 6, dT = L % 6;
    const int wf = wid & 1, kh = wid >> 1, r = lane & 31, h = lane >> 5, g = lane >> 4, q = (lane >> 2) & 3, p = lane & 3;
    const int f0 = ft * 128 + (wf * 2 + kh) * 32;
    u32x2 bb[4];
    UNR for (int m = 0; m < 4; m++) bb[m] = *(const u32x2*)(b1 + f0 + 8 * m + 4 * h);
    unsigned aoff[2];
    UNR for (int i = 0; i < 2; i++) aoff[i] = kh * 8192 + (4 * h + q) * 256 + ((((wf * 2 + (i ^ kh)) * 2 + (g & 1)) ^ (q << 1)) * 32) + p * 8;
    const unsigned boff = A_H + kh * 6144 + lane * 16;
    f32x16 acc[2][3];
    UNR for (int j = 0; j < 3; j++) { UNR for (int m = 0; m < 4; m++) { acc[0][j][4 * m] = blo(bb[m][0]); acc[0][j][4 * m + 1] = bhi(bb[m][0]); acc[0][j][4 * m + 2] = blo(bb[m][1]); acc[0][j][4 * m + 3] = bhi(bb[m][1]); } acc[1][j] = (f32x16){}; }
    bf16x8 xa[2], xb[3], ya[2] = {Z8, Z8}, yb[3] = {Z8, Z8, Z8};
#define ARD(A, B, ks) do { B[0] = rd128(bh + (ks) * 3072); A[1] = tr2(bw + aoff[1] + (ks) * 4096, 2048); B[1] = rd128(bh + (ks) * 3072 + 1024); \
        B[2] = rd128(bh + (ks) * 3072 + 2048); A[0] = tr2(bw + aoff[0] + (ks) * 4096, 2048); } while (0)
#define AMM(A, B) do { UNR for (int i = 1; i >= 0; i--) UNR for (int j = 0; j < 3; j++) acc[i][j] = MFMA32(A[i], B[j], acc[i][j]); } while (0)
#define ASG() do { UNR for (int i = 0; i < 3; i++) { SGB(0x8, 1); SGB(0x100, 3); } SGB(0x8, 6); } while (0)
    BAR();
    int slot = 0;
    ROLL for (int t = 0; t < 32; t++) {
        BAR(); if (t == 0) STAMP(10); LAS const char* bw = (LAS const char*)lds + slot * 16384; LAS const char* bh = (LAS const char*)lds + boff + slot * 12288;
        SB(); ARD(xa, xb, 0); AMM(ya, yb); ASG(); WAITL(); ARD(ya, yb, 1); AMM(xa, xb); ASG(); WAITL();
        slot = (slot == A_NS - 1) ? 0 : slot + 1;
    }
    BAR();
    AMM(ya, yb); STAMP(11);
    LAS f32x4* ex = (LAS f32x4*)lds + wid * 12 * 64 + lane;
    UNR for (int j = 0; j < 3; j++) UNR for (int m = 0; m < 4; m++) ex[(j * 4 + m) * 64] = (f32x4){acc[1][j][4 * m], acc[1][j][4 * m + 1], acc[1][j][4 * m + 2], acc[1][j][4 * m + 3]};
    unsigned uo[3];
    UNR for (int j = 0; j < 3; j++) { const int d = dT * 96 + j * 32 + r, dT9 = d / 144, dl = d - 144 * dT9;
        uo[j] = dT9 * (144 * 9216) + h * 256 + (dl < 128 ? (2 * (dl >> 5) + ((dl >> 2) & 1)) * 1024 + (4 * ((dl >> 3) & 3) + (dl & 3)) * 16 : 8192 + (dl - 128) * 16); }
    WAITL(); BAR(); STAMP(12);
    LAS const f32x4* ey = (LAS const f32x4*)lds + (wid ^ 2) * 12 * 64 + lane;
    f32x4 y[12];
    UNR for (int i = 0; i < 12; i++) y[i] = ey[i * 64];
    UNR for (int j = 0; j < 3; j++) {
        f32x2 u[8], t[8];
        UNR for (int m = 0; m < 4; m++) { u[2 * m] = (f32x2){acc[0][j][4 * m], acc[0][j][4 * m + 1]} + lo2(y[j * 4 + m]); u[2 * m + 1] = (f32x2){acc[0][j][4 * m + 2], acc[0][j][4 * m + 3]} + hi2(y[j * 4 + m]); }
        SB(); UNR for (int i = 0; i < 8; i++) t[i] = u[i] * u[i];
        SB(); UNR for (int i = 0; i < 8; i++) t[i] = pfma(t[i], splat(-2.f * 0.7978845608028654f * 0.044715f * 1.4426950408889634f), splat(-2.f * 0.7978845608028654f * 1.4426950408889634f));
        SB(); UNR for (int i = 0; i < 8; i++) t[i] = t[i] * u[i];
        SB(); UNR for (int i = 0; i < 8; i++) t[i] = (f32x2){__builtin_amdgcn_exp2f(t[i][0]), __builtin_amdgcn_exp2f(t[i][1])};
        SB(); UNR for (int i = 0; i < 8; i++) t[i] = t[i] + splat(1.f);
        SB(); UNR for (int i = 0; i < 8; i++) t[i] = (f32x2){__builtin_amdgcn_rcpf(t[i][0]), __builtin_amdgcn_rcpf(t[i][1])};
        SB(); UNR for (int i = 0; i < 8; i++) t[i] = t[i] * u[i];
        SB();
        UNR for (int s2 = 0; s2 < 2; s2++) st128((u32x4){cvt2(t[4 * s2]), cvt2(t[4 * s2 + 1]), cvt2(t[4 * s2 + 2]), cvt2(t[4 * s2 + 3])}, sw, uo[j] + s2 * 512, WS_UF + (f0 >> 5) * 9216);
    }
    STAMP(13);
    raise(sw, WS_F4 + ((ft * 6 + dT) * 4 + wid) * 4, lane); STAMP(24);
#undef ARD
#undef AMM
#undef ASG
}

constexpr int B_NS = 4, B_W = B_NS * 18432;
DI void down_loader(char* lds, char* ws, const bf16_t* w2, srd_t sw, int bid, int lw, int lane PROFARG) {
    const int xg = bid & 7, idx = bid >> 3, kq = xg >> 1, st = (xg & 1) * 8 + (idx >> 2), dT = idx & 3, rq = lane >> 4, c = lane & 15; const unsigned lds0 = (unsigned)(uintptr_t)lds;
    const srd_t s2 = mksrd(w2, S * F * 2), su = mksrd(ws + WS_UF, D * F * 2);
    unsigned vw[2]; const unsigned vu = lane * 16;
    UNR for (int e = 0; e < 2; e++) vw[e] = (kq * 1152 + 16 * lw + rq) * (S * 2) + st * 256 + ((((c >> 1) ^ (4 * e + rq)) * 2) + (c & 1)) * 16;
    auto iw = [&](int t, int slot) { UNR for (int i = 0; i < 4; i++) dma(lds0 + B_W + slot * 16384 + (16 * lw + 4 * i) * 256, vw[i & 1], s2, (t * 64 + 4 * i) * (S * 2)); };
    UNR for (int t = 0; t < B_NS; t++) iw(t, t);
    if (lw == 0) await4(sw, lane < 18 ? WS_F4 + ((kq * 9 + (lane >> 1)) * 6 + ((3 * dT) >> 1) + (lane & 1)) * 16 : OOB);
    BAR(); STAMP(25);
    auto run = [&](auto NU) {
        constexpr int nu = NU.value; const int u0 = nu == 5 ? lw * 5 : 2 + lw * 4;
        ring_rest<18, B_NS, 4, nu>(iw, [&](int t, int slot) { UNR for (int j = 0; j < nu; j++) dma_dev(lds0 + slot * 18432 + (u0 + j) * 1024, vu, su, (dT * 144 + kq * 36 + 2 * t) * 9216 + (u0 + j) * 1024); }); };
    if (lw < 2) run(IC<5>{}); else run(IC<4>{});
}
DI void down_mfma(char* lds, char* ws, const bf16_t* b2, bf16_t* out, srd_t sw, int bid, int wid, int lane PROFARG) {
    const int xg = bid & 7, idx = bid >> 3, kq = xg >> 1, st = (xg & 1) * 8 + (idx >> 2), dT = idx & 3, tile = st * 4 + dT;
    const int s = lane & 15, g = lane >> 4, q = (lane >> 2) & 3, p = lane & 3, r7 = 4 * (g & 1) + q;
    unsigned boff[2];
    UNR for (int j = 0; j < 2; j++) boff[j] = B_W + (16 * (g >> 1) + r7) * 256 + (((wid * 2 + (j ^ (kq >> 1))) ^ r7) * 32) + p * 8;
    const unsigned aoff = lane * 16; const int asw = (kq & 1) * 4096;
    f32x4 acc[9][2];
    UNR for (int i = 0; i < 9; i++) UNR for (int j = 0; j < 2; j++) acc[i][j] = (f32x4){0.f, 0.f, 0.f, 0.f};
    bf16x8 xa[9], xb[2], ya[9], yb[2] = {Z8, Z8};
    UNR for (int i = 0; i < 9; i++) ya[i] = Z8;
#define BRD(A, B, ks) do { B[0] = tr2(bw + boff[0] + (ks) * 8192, 2048); A[0] = rd128(ua + (ks) * 9216); B[1] = tr2(bw + boff[1] + (ks) * 8192, 2048); \
        UNR for (int i = 1; i < 9; i++) A[i] = rd128((i < 4 ? ua : i < 8 ? ub : uc) + (ks) * 9216 + i * 1024); } while (0)
#define BMM(A, B) do { UNR for (int i = 0; i < 9; i++) UNR for (int j = 0; j < 2; j++) acc[i][j] = MFMA16(A[i], B[j], acc[i][j]); } while (0)
#define BSG() do { UNR for (int i = 0; i < 7; i++) { SGB(0x8, 1); SGB(0x100, 2); } SGB(0x8, 18); } while (0)
    BAR();
    int slot = 0;
    ROLL for (int t = 0; t < 18; t++) {
        BAR(); if (t == 0) STAMP(14);
        LAS const char* bw = (LAS const char*)lds + slot * 16384; LAS const char* uc = (LAS const char*)lds + aoff + slot * 18432; LAS const char* ua = uc + asw; LAS const char* ub = uc - asw;
        SB(); BRD(xa, xb, 0); BMM(ya, yb); BSG(); WAITL(); BRD(ya, yb, 1); BMM(xa, xb); BSG(); WAITL();
        slot = (slot + 1) & 3;
    }
    BMM(ya, yb); STAMP(15);
    const bool odd = kq & 1; const int s16 = st * 8 + wid * 2 + (kq >> 1);
    gu32* fl = (gu32*)(ws + WS_F5) + (tile * 4 + wid) * 32;
    const unsigned box = (tile * 4 + wid) * 12, v16 = lane * 16, v8e = odd ? OOB : 2048 + lane * 8, v8o = odd ? 2048 + lane * 8 : OOB;
#define PK2(i, j) (u32x4){cvt2(lo2(acc[i][j])), cvt2(hi2(acc[i][j])), cvt2(lo2(acc[(i) + 1][j])), cvt2(hi2(acc[(i) + 1][j]))}
#define PK1(j) (u32x2){cvt2(lo2(acc[8][j])), cvt2(hi2(acc[8][j]))}
    const unsigned o1 = WS_PX + (box + (kq ^ 1) * 3) * 2560, o2 = WS_PX + (box + (kq ^ 2) * 3 + 1) * 2560, o3 = WS_PX + (box + (kq ^ 3) * 3 + 2) * 2560;
    st128(PK2(4, 0), sw, v16, o1); st128(PK2(6, 0), sw, v16 + 1024, o1); st128(PK2(0, 1), sw, v16, o2); st128(PK2(2, 1), sw, v16 + 1024, o2);
    st128(PK2(4, 1), sw, v16, o3); st128(PK2(6, 1), sw, v16 + 1024, o3); st64(PK1(0), sw, v8e, o1); st64(PK1(1), sw, 2048 + lane * 8, odd ? o2 : o3);
    const unsigned hf = WS_HF + s16 * 18432 + dT * 4608;
    const u32x4 hp0 = ld128(sw, v16, hf + (kq & 1) * 2048), hp1 = ld128(sw, v16 + 1024, hf + (kq & 1) * 2048);
    const u32x2 hs = ld64(sw, lane * 8, hf + 4096); const float bs = blo(b2[s16 * 16 + s]);
    waitv<0>(); STAMP(16);
    if (lane == 0) __hip_atomic_fetch_add(fl, 1u, RLX_AGENT);
    f32x2 r[5][2];
    UNR for (int i = 0; i < 4; i++) { const u32x4 hh = i < 2 ? hp0 : hp1; r[i][0] = lo2(acc[i][0]) + splat(bs) + unpk(hh[2 * (i & 1)]); r[i][1] = hi2(acc[i][0]) + splat(bs) + unpk(hh[2 * (i & 1) + 1]); }
    r[4][0] = lo2(acc[8][0]) + splat(bs) + unpk(hs[0]); r[4][1] = hi2(acc[8][0]) + splat(bs) + unpk(hs[1]);
    for (unsigned spins = 0; rfl(__hip_atomic_load(fl, RLX_AGENT)) < 4u && spins < SPIN_CAP; spins++) __builtin_amdgcn_s_sleep(1);
    STAMP(17);
    asm volatile("" ::: "memory");
    u32x4 pa[3][2]; u32x2 pb[3];
    UNR for (int c = 0; c < 3; c++) { const unsigned o = WS_PX + (box + kq * 3 + c) * 2560; pa[c][0] = ld128(sw, v16, o); pa[c][1] = ld128(sw, v16 + 1024, o); pb[c] = ld64(sw, v8o, o); }
    SB();
    UNR for (int c = 0; c < 3; c++) {
        UNR for (int i = 0; i < 4; i++) { r[i][0] += unpk(pa[c][i >> 1][2 * (i & 1)]); r[i][1] += unpk(pa[c][i >> 1][2 * (i & 1) + 1]); }
        r[4][0] += unpk(pb[c][0]); r[4][1] += unpk(pb[c][1]);
    }
    STAMP(18);
    const srd_t so = mksrd(out, S * D * 2); const unsigned ob = (s16 * 16 + s) * 1152 + dT * 288;
    __builtin_amdgcn_raw_buffer_store_b128((u32x4){cvt2(r[0][0]), cvt2(r[0][1]), cvt2(r[1][0]), cvt2(r[1][1])}, so, ob + (kq & 1) * 128 + 16 * g, 0, 0);
    __builtin_amdgcn_raw_buffer_store_b128((u32x4){cvt2(r[2][0]), cvt2(r[2][1]), cvt2(r[3][0]), cvt2(r[3][1])}, so, ob + (kq & 1) * 128 + 64 + 16 * g, 0, 0);
    __builtin_amdgcn_raw_buffer_store_b64((u32x2){cvt2(r[4][0]), cvt2(r[4][1])}, so, odd ? ob + 256 + 8 * g : OOB, 0, 0);
#undef PK2
#undef PK1
#undef BRD
#undef BMM
#undef BSG
}

__global__ __launch_bounds__(768, 3) void k_block(const bf16_t* __restrict__ x, const bf16_t* __restrict__ wqkv, const bf16_t* __restrict__ wo, const bf16_t* __restrict__ w1, const bf16_t* __restrict__ b1,
                                                  const bf16_t* __restrict__ w2, const bf16_t* __restrict__ b2, bf16_t* __restrict__ out, char* __restrict__ ws) {
    __shared__ __attribute__((aligned(16))) char lds[LDS_BYTES]; PROFILE(0);
    const int tid = threadIdx.x, lane = tid & 63, wid = rfl(tid >> 6), bid = blockIdx.x;
    const srd_t sw = mksrd(ws, WS_BYTES);
    u32x2 ep = {0u, 0u};
    if (wid == 0 || wid == 8) ep = ld64(sw, 0, WS_EP);
    phase_qkv(lds, x, wqkv, sw, bid, tid, wid, lane PROFPASS);
    const unsigned long long tag = (((unsigned long long)rfl(ep[1]) << 32) | rfl(ep[0])) + 1ull;
    STAMP(19); waitv<0>(); STAMP(20); BAR();
    if (wid == 0) st64((u32x2){(unsigned)tag, (unsigned)(tag >> 32)}, sw, lane == 0 ? bid * 8 : OOB, WS_F1);
    if (wid > 8) return;
    if (wid == 8) { attn_loader(lds, ws, wo, w1, w2, sw, tag, bid, lane PROFPASS); return; }
    attn_compute(lds, ws, sw, bid, wid, lane PROFPASS);
    const unsigned f2 = WS_F2 + (((bid >> 5) * 8 + wid) * 32 + (bid & 7) * 4 + ((bid >> 3) & 3)) * 4;
    if (wid < 4) {
        raise(sw, f2, lane); STAMP(21);
        if (bid < 192) wo_mfma(lds, ws, sw, bid, wid, lane PROFPASS);
        if (bid < 216) up_mfma(lds, ws, b1, sw, bid, wid, lane PROFPASS);
        down_mfma(lds, ws, b2, out, sw, bid, wid, lane PROFPASS);
        if (bid == 0 && wid == 0) st64((u32x2){(unsigned)tag, (unsigned)(tag >> 32)}, sw, lane == 0 ? 0u : OOB, WS_EP);
    } else {
        if (bid < 192) wo_loader(lds, x, wo, sw, f2, bid, wid - 4, lane); else raise(sw, f2, lane);
        if (bid < 216) up_loader(lds, ws, w1, sw, bid, wid - 4, lane PROFPASS);
        down_loader(lds, ws, w2, sw, bid, wid - 4, lane PROFPASS);
    }
}

extern "C" void kernel_launch(void* const* d_in, const int* in_sizes, int n_in, void* d_out, int out_size, void* d_ws, size_t ws_size, hipStream_t stream) {
    k_block<<<256, 768, 0, stream>>>((const bf16_t*)d_in[0], (const bf16_t*)d_in[1], (const bf16_t*)d_in[2], (const bf16_t*)d_in[4], (const bf16_t*)d_in[5], (const bf16_t*)d_in[6], (const bf16_t*)d_in[7],
                                     (bf16_t*)d_out, (char*)d_ws);
}
```

```cpp
#include <hip/hip_runtime.h>
#include <stdint.h>
#include <type_traits>

typedef unsigned short bf16_t;
typedef short bf16x8 __attribute__((ext_vector_type(8)));
typedef short s16x4 __attribute__((ext_vector_type(4)));
typedef float f32x2 __attribute__((ext_vector_type(2)));
typedef float f32x4 __attribute__((ext_vector_type(4)));
typedef float f32x16 __attribute__((ext_vector_type(16)));
typedef unsigned u32x2 __attribute__((ext_vector_type(2)));
typedef unsigned u32x4 __attribute__((ext_vector_type(4)));
typedef __amdgpu_buffer_rsrc_t srd_t;
#define LAS __attribute__((address_space(3)))
#define DI __device__ __forceinline__

constexpr int S = 2048, D = 576, F = 4608;
constexpr int CHUNK = 18944, BLOB = 8 * CHUNK;
constexpr unsigned WS_QB = 0;
constexpr unsigned WS_QR = WS_QB + 32 * 64 * 1024;
constexpr unsigned WS_KV = WS_QR + 32 * 2048 * 4;
constexpr unsigned WS_OF = WS_KV + 32 * BLOB;
constexpr unsigned WS_HF = WS_OF + S * D * 2;
constexpr unsigned WS_HH = WS_HF + S * D * 2;
constexpr unsigned WS_UF = WS_HH + S * D * 2;
constexpr unsigned WS_ST = WS_UF + D * F * 2;
constexpr unsigned WS_F3 = WS_ST + S * 6 * 8;
constexpr unsigned WS_F4 = WS_F3 + 384 * 128;
constexpr unsigned WS_FW = WS_F4 + 160 * 128;
constexpr unsigned WS_F1 = WS_FW + 32 * 128;
constexpr unsigned WS_S1 = WS_F1 + 2048 * 128;
constexpr unsigned WS_C1 = WS_S1 + S * 16;
constexpr unsigned WS_WP = WS_C1 + 1792 * 8;
constexpr unsigned WS_EP = WS_WP + D * D * 2;
constexpr unsigned WS_PX = WS_EP + 128;
constexpr unsigned PX_BYTES = 64 * 4 * 4 * 3 * 2560, WS_BYTES = WS_PX + PX_BYTES, OOB = 0x80000000u;
constexpr int LDS_BYTES = BLOB + 10240;
constexpr float EPS = 1e-5f;
constexpr float QSCALE = 0.23570226039551584f * 1.4426950408889634f;
constexpr unsigned SPIN_CAP = 100000u, ONES = 0x3f803f80u;
constexpr int SC1 = 16;

DI unsigned rfl(unsigned x) { return __builtin_amdgcn_readfirstlane(x); }
DI srd_t mksrd(const void* p, unsigned bytes) { return __builtin_amdgcn_make_buffer_rsrc((void*)p, (short)0, (int)bytes, 0x00020000); }
DI void dma(unsigned ldsaddr, unsigned voff, srd_t srd, unsigned soff) {
    asm volatile("s_mov_b32 m0, %0\n\ts_nop 0\n\tbuffer_load_dwordx4 %1, %2, %3 offen lds" ::"s"(ldsaddr), "v"(voff), "s"(srd), "s"(soff) : "memory");
}
DI void dma_dev(unsigned ldsaddr, unsigned voff, srd_t srd, unsigned soff) {
    asm volatile("s_mov_b32 m0, %0\n\ts_nop 0\n\tbuffer_load_dwordx4 %1, %2, %3 offen sc1 lds" ::"s"(ldsaddr), "v"(voff), "s"(srd), "s"(soff) : "memory");
}
DI void touch(unsigned& sink, unsigned voff, srd_t srd, unsigned soff) {
    asm volatile("buffer_load_dword %0, %1, %2, %3 offen\n\ts_waitcnt vmcnt(3)" : "+v"(sink) : "v"(voff), "s"(srd), "s"(soff) : "memory");
}
DI s16x4 trrd(LAS const char* p) { return __builtin_amdgcn_ds_read_tr16_b64_v4i16((LAS s16x4*)p); }
DI bf16x8 rd128(LAS const char* p) { return *(LAS const bf16x8*)p; }
DI bf16x8 cat(s16x4 a, s16x4 b) { return __builtin_shufflevector(a, b, 0, 1, 2, 3, 4, 5, 6, 7); }
DI bf16x8 tr2(LAS const char* p, int second) { return cat(trrd(p), trrd(p + second)); }
typedef __bf16 bfx2 __attribute__((ext_vector_type(2)));
DI unsigned cvtpk(float lo, float hi) { return __builtin_bit_cast(unsigned, __builtin_convertvector((f32x2){lo, hi}, bfx2)); }
DI float blo(unsigned x) { return __uint_as_float(x << 16); }
DI float bhi(unsigned x) { return __uint_as_float(x & 0xffff0000u); }
DI float rb(float x) { return blo(cvtpk(x, 0.f)); }
DI float swapadd32(float v) { auto r = __builtin_amdgcn_permlane32_swap(__float_as_uint(v), __float_as_uint(v), false, false); return __uint_as_float(r[0]) + __uint_as_float(r[1]); }
DI float swapmax32(float v) { auto r = __builtin_amdgcn_permlane32_swap(__float_as_uint(v), __float_as_uint(v), false, false); return __builtin_fmaxf(__uint_as_float(r[0]), __uint_as_float(r[1])); }
DI float lowhalf(float v) { auto r = __builtin_amdgcn_permlane32_swap(__float_as_uint(v), __float_as_uint(v), false, false); return __uint_as_float(r[0]); }
DI float swapadd16(float v) { auto r = __builtin_amdgcn_permlane16_swap(__float_as_uint(v), __float_as_uint(v), false, false); return __uint_as_float(r[0]) + __uint_as_float(r[1]); }
DI f32x2 pfma(f32x2 a, f32x2 b, f32x2 c) { return __builtin_elementwise_fma(a, b, c); }
DI f32x2 splat(float a) { return (f32x2){a, a}; }
DI f32x2 lo2(f32x4 v) { return (f32x2){v[0], v[1]}; }
DI f32x2 hi2(f32x4 v) { return (f32x2){v[2], v[3]}; }
DI f32x2 unpk(unsigned x) { return (f32x2){blo(x), bhi(x)}; }
DI unsigned cvt2(f32x2 v) { return cvtpk(v[0], v[1]); }
DI void st128(u32x4 v, srd_t r, unsigned vo, unsigned so) { __builtin_amdgcn_raw_buffer_store_b128(v, r, vo, so, SC1); asm volatile("s_nop 1" ::"v"(v)); }
DI void st64(u32x2 v, srd_t r, unsigned vo, unsigned so) { __builtin_amdgcn_raw_buffer_store_b64(v, r, vo, so, SC1); }
DI void st32(unsigned v, srd_t r, unsigned vo, unsigned so) { __builtin_amdgcn_raw_buffer_store_b32(v, r, vo, so, SC1); }
DI u32x4 ld128(srd_t r, unsigned vo, unsigned so) { return __builtin_amdgcn_raw_buffer_load_b128(r, vo, so, SC1); }
DI u32x2 ld64(srd_t r, unsigned vo, unsigned so) { return __builtin_amdgcn_raw_buffer_load_b64(r, vo, so, SC1); }
DI unsigned ld32(srd_t r, unsigned vo, unsigned so) { return __builtin_amdgcn_raw_buffer_load_b32(r, vo, so, SC1); }
#define MFMA16(a, b, c) __builtin_amdgcn_mfma_f32_16x16x32_bf16(a, b, c, 0, 0, 0)
#define MFMA32(a, b, c) __builtin_amdgcn_mfma_f32_32x32x16_bf16(a, b, c, 0, 0, 0)
#define MFMA32K8(a, b, c) __builtin_amdgcn_mfma_f32_32x32x8bf16_1k(a, b, c, 0, 0, 0)
#define BAR() asm volatile("s_barrier" ::: "memory")
#define SB() __builtin_amdgcn_sched_barrier(0)
#define SGB(m, n) __builtin_amdgcn_sched_group_barrier(m, n, 0)
#define WAITL() do { SB(); __builtin_amdgcn_s_waitcnt(0xC07F); SB(); } while (0)
#define UNR _Pragma("unroll")
#define ROLL _Pragma("unroll 1")
template <int N> DI void waitv() { asm volatile("s_waitcnt vmcnt(%0)" ::"i"(N) : "memory"); }
#define Z8 ((bf16x8){0, 0, 0, 0, 0, 0, 0, 0})
template <int N> using IC = std::integral_constant<int, N>;

DI void arrive4(char* lds, int word, srd_t sw, unsigned voff, unsigned soff, int lane) {
    waitv<0>(); unsigned old = 0;
    if (lane == 0) old = __hip_atomic_fetch_add((LAS unsigned*)(lds + LDS_BYTES - 16) + word, 1u, __ATOMIC_RELAXED, __HIP_MEMORY_SCOPE_WORKGROUP);
    if (rfl(old) == 3u) __builtin_amdgcn_raw_ptr_buffer_atomic_add_i32(4, sw, voff, soff, 0);
}
DI void await(srd_t sw, unsigned voff, unsigned soff, unsigned need) {
    ROLL for (unsigned spins = 0; spins < SPIN_CAP; spins++) {
        asm volatile("" ::: "memory");
        const unsigned v = ld32(sw, voff, soff);
        if (__all(voff == OOB || v >= need)) break;
    }
    asm volatile("" ::: "memory");
}
DI void await_first(srd_t sw, unsigned soff) {
    ROLL for (unsigned spins = 0; spins < SPIN_CAP; spins++) { asm volatile("" ::: "memory"); if (rfl(ld32(sw, 0, soff))) break; __builtin_amdgcn_s_sleep(16); }
}
DI u32x2 granule(float v, unsigned tl, unsigned th) { return (u32x2){((__float_as_uint(v) + 128u) >> 8) | (tl << 24), (tl >> 8) | (th << 24)}; }
template <int NT, int NS, int LAG, int PW, int PA, class IW, class IA> DI void ring_rest(IW iw, IA ia) {
    constexpr int P = PW + PA, FL = NS - 2 - LAG;
    UNR for (int t = 0; t < NS; t++) ia(t, t);
    auto head = [&](auto T) { constexpr int t = T.value; waitv<(NS - 1 - t) * PA + (t > LAG + 1 ? t - LAG - 1 : 0) * P>(); BAR(); if constexpr (t > LAG) { iw(t + NS - 1 - LAG, t - 1 - LAG); ia(t + NS - 1 - LAG, t - 1 - LAG); } };
    head(IC<0>{}); head(IC<1>{}); head(IC<2>{});
    if constexpr (NS == 5) head(IC<3>{});
    int slot = FL;
    ROLL for (int t = NS - 1; t <= NT - NS + LAG; t++) { waitv<FL * P>(); BAR(); iw(t + NS - 1 - LAG, slot); ia(t + NS - 1 - LAG, slot); slot = (slot == NS - 1) ? 0 : slot + 1; }
    if constexpr (FL == 3) { waitv<3 * P>(); BAR(); }
    waitv<2 * P>(); BAR(); waitv<P>(); BAR(); waitv<0>(); BAR();
}

constexpr int Q_STG = 32768, Q_NS = 4, Q_ST = Q_NS * Q_STG, Q_TAB = Q_ST + 1536;
template <bool VT> DI void qkv_mfma(char* lds, srd_t sw, u32x2 ep, int mt, int nt, int wid, int lane) {
    const int wn = wid & 1, wsn = wid >> 1, r = lane & 31, h = lane >> 5, g = lane >> 4, q = (lane >> 2) & 3, p = lane & 3;
    unsigned xoff[2][2], woff[2];
    UNR for (int b = 0; b < 2; b++) { const int row = wsn * 64 + b * 32 + r; UNR for (int e = 0; e < 2; e++) xoff[b][e] = row * 128 + (((2 * e + h) ^ ((row >> 1) & 3)) | (((row >> 1) & 4))) * 16; }
    UNR for (int a = 0; a < 2; a++) woff[a] = 16384 + (8 * h + q) * 256 + ((((wn * 2 + a) * 2 + (g & 1)) ^ (q << 1)) * 32) + p * 8;
    f32x16 acc[2][2];
    UNR for (int a = 0; a < 2; a++) UNR for (int b = 0; b < 2; b++) acc[a][b] = (f32x16){};
    bf16x8 xw[2][2], xx[2][2], yw[2][2] = {{Z8, Z8}, {Z8, Z8}}, yx[2][2] = {{Z8, Z8}, {Z8, Z8}};
#define QRD(W, X, hf) do { UNR for (int e = 0; e < 2; e++) { W[e][0] = tr2(base + woff[0] + (2 * (hf) + e) * 4096, 1024); X[e][0] = rd128(base + (xoff[0][e] ^ ((hf) * 64))); \
        X[e][1] = rd128(base + (xoff[1][e] ^ ((hf) * 64))); W[e][1] = tr2(base + woff[1] + (2 * (hf) + e) * 4096, 1024); } } while (0)
#define QMM(W, X) do { UNR for (int e = 0; e < 2; e++) UNR for (int a = 0; a < 2; a++) UNR for (int b = 0; b < 2; b++) acc[a][b] = VT ? MFMA32(X[e][b], W[e][a], acc[a][b]) : MFMA32(W[e][a], X[e][b], acc[a][b]); } while (0)
#define QSG() do { UNR for (int i = 0; i < 6; i++) { SGB(0x8, 1); SGB(0x100, 2); } SGB(0x8, 8); } while (0)
#define QSTAGE(slot) do { LAS const char* base = (LAS const char*)lds + (slot) * Q_STG; \
        SB(); QRD(xw, xx, 0); QMM(yw, yx); QSG(); SB(); QRD(yw, yx, 1); QMM(xw, xx); QSG(); WAITL(); } while (0)
    { int sl = 0; ROLL for (int t = 0; t < 8; t++) { BAR(); QSTAGE(sl); sl = (sl + 1) & 3; } }
    const unsigned go = h ? WS_C1 + (nt * 128 + wid * 32 + r) * 8 : WS_S1 + (mt * 128 + wid * 32 + r) * 16;
    u32x4 gv = ld128(sw, go, 0);
    BAR(); QSTAGE(0);
    QMM(yw, yx);
    { const unsigned long long tag = (((unsigned long long)rfl(ep[1]) << 32) | rfl(ep[0])) + 1ull; const u32x2 want = granule(0.f, (unsigned)tag, (unsigned)(tag >> 32));
      const bool idle = h && nt * 128 + wid * 32 >= 3 * D;
      ROLL for (unsigned spins = 0; spins < SPIN_CAP; spins++) {
          if (__all(idle || ((gv[0] >> 24) == (want[0] >> 24) && gv[1] == want[1] && (h || ((gv[2] >> 24) == (want[0] >> 24) && gv[3] == want[1]))))) break;
          asm volatile("" ::: "memory"); gv = ld128(sw, go, 0);
      }
      LAS float* st = (LAS float*)(lds + Q_ST) + h * 256 + wid * 32 + r;
      st[0] = __uint_as_float(gv[0] << 8); if (!h) st[128] = __uint_as_float(gv[2] << 8); }
    WAITL(); BAR();
    LAS const float* mu = (LAS const float*)(lds + Q_ST); LAS const float* rs = mu + 128; LAS const float* cs = mu + 256;
    if constexpr (!VT) {
        const float sc = nt * 128 + wn * 64 >= D ? 1.f : QSCALE;
        f32x2 m_[2], r_[2];
        UNR for (int b = 0; b < 2; b++) { m_[b] = splat(-mu[wsn * 64 + b * 32 + r]); r_[b] = splat(rs[wsn * 64 + b * 32 + r] * sc); }
        LAS char* img = (LAS char*)lds + (wsn * 64 + wn * 16 + h) * 256 + r * 8;
        f32x4 ca[8];
        UNR for (int i = 0; i < 8; i++) ca[i] = *(LAS const f32x4*)(cs + wn * 64 + 8 * i + 4 * h);
        SB();
        UNR for (int a = 0; a < 2; a++) UNR for (int m = 0; m < 4; m++) {
            const f32x4 c4 = ca[4 * a + m];
            UNR for (int b = 0; b < 2; b++)
                *(LAS u32x2*)(img + (b * 32 + 8 * a + 2 * m) * 256) = (u32x2){cvt2(pfma(m_[b], lo2(c4), (f32x2){acc[a][b][4 * m], acc[a][b][4 * m + 1]}) * r_[b]),
                                                                             cvt2(pfma(m_[b], hi2(c4), (f32x2){acc[a][b][4 * m + 2], acc[a][b][4 * m + 3]}) * r_[b])};
        }
    } else {
        LAS char* vo[2]; f32x2 c1[2];
        const int col0 = nt * 128 - 2 * D, head0 = (col0 * 3641) >> 16;
        UNR for (int a = 0; a < 2; a++) {
            const int nl = wn * 64 + a * 32 + r, col = col0 + nl, head = (col * 3641) >> 16;
            vo[a] = (LAS char*)lds + (head - head0) * (4864 - 18 * 16) + (col - 18 * head0) * 16 + h * 304 + wsn * 2432; c1[a] = splat(-cs[nl]);
        }
        f32x4 ma[8], ra[8];
        UNR for (int i = 0; i < 8; i++) { ma[i] = *(LAS const f32x4*)(mu + wsn * 64 + 8 * i + 4 * h); ra[i] = *(LAS const f32x4*)(rs + wsn * 64 + 8 * i + 4 * h); }
        SB();
        UNR for (int b = 0; b < 2; b++) {
            UNR for (int s2 = 0; s2 < 2; s2++) {
                const f32x4* m4 = ma + 4 * b + 2 * s2; const f32x4* r4 = ra + 4 * b + 2 * s2;
                UNR for (int a = 0; a < 2; a++) {
                    u32x4 o;
                    UNR for (int e2 = 0; e2 < 2; e2++) { const int i = 8 * s2 + 4 * e2;
                        o[2 * e2] = cvt2(pfma(lo2(m4[e2]), c1[a], (f32x2){acc[a][b][i], acc[a][b][i + 1]}) * lo2(r4[e2]));
                        o[2 * e2 + 1] = cvt2(pfma(hi2(m4[e2]), c1[a], (f32x2){acc[a][b][i + 2], acc[a][b][i + 3]}) * hi2(r4[e2])); }
                    *(LAS u32x4*)(vo[a] + b * 1216 + s2 * 608) = o;
                }
            }
        }
    }
#undef QRD
#undef QMM
#undef QSG
#undef QSTAGE
}
DI float sum8(float v) { v += __builtin_amdgcn_update_dpp(0.f, v, 0xB1, 0xF, 0xF, true); v += __builtin_amdgcn_update_dpp(0.f, v, 0x4E, 0xF, 0xF, true); return v + __builtin_amdgcn_update_dpp(0.f, v, 0x141, 0xF, 0xF, true); }
DI void phase_spare(char* lds, const bf16_t* x, const bf16_t* w, srd_t sw, u32x2 ep, int j, int tid, int wid, int lane) {
    const srd_t sx = mksrd(x, S * D * 2), sq = mksrd(w, D * 3 * D * 2);
    const int rl = lane >> 3, c = lane & 7, row = j * 64 + wid * 8 + rl;
    u32x4 xv[9], wv[9];
    UNR for (int i = 0; i < 9; i++) xv[i] = __builtin_amdgcn_raw_buffer_load_b128(sx, row * 1152 + c * 16, i * 128, 0);
    UNR for (int i = 0; i < 9; i++) wv[i] = __builtin_amdgcn_raw_buffer_load_b128(sq, j < 27 ? (wid * 72 + rl) * 3456 + j * 128 + c * 16 : OOB, i * 8 * 3456, 0);
    st128((u32x4){~0u, ~0u, ~0u, ~0u}, sw, tid < 192 ? tid * 16 : OOB, WS_ST + j * 3072);
    st128((u32x4){0u, 0u, 0u, 0u}, sw, tid < 136 ? tid * 16 : OOB, WS_F3 + j * 2176);
    const unsigned long long tag = (((unsigned long long)rfl(ep[1]) << 32) | rfl(ep[0])) + 1ull; const unsigned tl = (unsigned)tag, th = (unsigned)(tag >> 32);
    f32x2 a = {0.f, 0.f};
    UNR for (int i = 0; i < 9; i++) UNR for (int e = 0; e < 4; e++) a += unpk(xv[i][e]);
    const float m = sum8(a[0] + a[1]) * (1.f / D);
    a = (f32x2){0.f, 0.f};
    UNR for (int i = 0; i < 9; i++) UNR for (int e = 0; e < 4; e++) { const f32x2 d = unpk(xv[i][e]) - splat(m); a = pfma(d, d, a); }
    const float rs = __builtin_amdgcn_rcpf(rb(__builtin_amdgcn_sqrtf(rb(rb(sum8(a[0] + a[1]) * (1.f / D)) + EPS))));
    st64(granule(c ? rs : rb(m), tl, th), sw, c < 2 ? row * 16 + c * 8 : OOB, WS_S1);
    if (j < 27) {
        f32x2 cs[4] = {{0.f, 0.f}, {0.f, 0.f}, {0.f, 0.f}, {0.f, 0.f}};
        UNR for (int i = 0; i < 9; i++) UNR for (int e = 0; e < 4; e++) cs[e] += unpk(wv[i][e]);
        LAS f32x4* pp = (LAS f32x4*)lds + (wid * 8 + rl) * 16 + c * 2;
        pp[0] = (f32x4){cs[0][0], cs[0][1], cs[1][0], cs[1][1]}; pp[1] = (f32x4){cs[2][0], cs[2][1], cs[3][0], cs[3][1]};
        WAITL(); BAR();
        float t = 0.f;
        UNR for (int i = 0; i < 8; i++) t += ((LAS const float*)lds)[(c * 8 + i) * 64 + wid * 8 + rl];
        st64(granule(sum8(t), tl, th), sw, c == 0 ? (j * 64 + wid * 8 + rl) * 8 : OOB, WS_C1);
    }
}
DI void spare_image(const bf16_t* wo, srd_t sw, unsigned tl, unsigned th, int j, int tid, int wid, int lane) {
    const srd_t so = mksrd(wo, D * D * 2); u32x4 pv[3];
    UNR for (int k = 0; k < 3; k++) {
        const int il = tid + 512 * k, it = j * 1296 + il, nt = it / 6912, rm = it - 6912 * nt, rr = rm / 12, cc = rm - 12 * rr, t = rr >> 6, kb = (rr >> 5) & 1, g = (rr >> 2) & 3, jj = (rr & 3) + 4 * ((rr >> 4) & 1);
        const int kk = t < 8 ? 72 * t + 36 * kb + 18 * (g >> 1) + 4 * (g & 1) + (jj & 3) + 8 * (jj >> 2) : 18 * (16 * kb + 4 * g + (jj >> 1)) + 16 + (jj & 1);
        pv[k] = __builtin_amdgcn_raw_buffer_load_b128(so, il < 1296 ? kk * 1152 + nt * 192 + (cc ^ ((g & 1) * 2)) * 16 : OOB, 0, 0);
    }
    UNR for (int k = 0; k < 3; k++) st128(pv[k], sw, tid + 512 * k < 1296 ? (tid + 512 * k) * 16 : OOB, WS_WP + j * 20736);
    waitv<0>(); BAR();
    if (wid == 0) st64((u32x2){tl, th}, sw, lane == 0 ? 0u : OOB, WS_FW + j * 128);
}
DI u32x2 image_flags(srd_t sw, int lane) { asm volatile("" ::: "memory"); return ld64(sw, (lane & 31) * 128, WS_FW); }
DI void phase_qkv(char* lds, const bf16_t* x, const bf16_t* w, srd_t sw, u32x2 ep, int bid, int tid, int wid, int lane) {
    const int xg = bid & 7, idx = bid >> 3, mt = (xg >> 1) * 4 + (idx & 3), nt = (xg & 1) * 7 + (idx >> 2);
    const unsigned lds0 = (unsigned)(uintptr_t)lds;
    if (wid < 4) { if (nt >= 9) qkv_mfma<true>(lds, sw, ep, mt, nt, wid, lane); else qkv_mfma<false>(lds, sw, ep, mt, nt, wid, lane); }
    else {
        const int lw = wid - 4; const srd_t sx = mksrd(x, S * D * 2), sq = mksrd(w, D * 3 * D * 2);
        unsigned vx[2];
        UNR for (int e = 0; e < 2; e++) vx[e] = (mt * 128 + 32 * lw + (lane >> 3)) * 1152 + ((lane & 7) ^ (4 * e + (lane >> 4))) * 16;
        const int rq = lane >> 4, c = lane & 15; const unsigned vw = (16 * lw + rq) * 3456 + nt * 256 + ((((c >> 1) ^ (rq << 1)) * 2) + (c & 1)) * 16;
        auto issue = [&](int t, int slot) {
            const unsigned lb = lds0 + slot * Q_STG;
            UNR for (int i = 0; i < 4; i++) dma(lb + 16384 + (16 * lw + 4 * i) * 256, vw, sq, (t * 64 + 4 * i) * 3456);
            UNR for (int i = 0; i < 4; i++) dma(lb + (32 * lw + 8 * i) * 128, vx[i & 1], sx, 8 * i * 1152 + t * 128);
        };
        issue(0, 0); issue(1, 1); waitv<8>(); BAR(); issue(2, 2); issue(3, 3);
        if (lw < 2) {
            const int T = mt * 4 + lw * 2 + (lane >> 5), n = nt * 128 + (lane & 31) * 4; const bool isk = n >= D;
            const int Qd = (n - (isk ? D : 0)) >> 2, ph = (Qd * 57) >> 9, qm = Qd - 9 * ph, head = 2 * ph + (qm > 4), pq = qm > 4 ? qm - 5 : qm;
            const unsigned kb = WS_KV + head * BLOB + (T >> 3) * CHUNK;
            const unsigned em = (isk ? kb + (T & 7) * 1024 : WS_QB + (head * 64 + T) * 1024) + (pq & 1) * 512 + (pq >> 1) * 256, er = isk ? kb + 8192 + (T & 7) * 128 : WS_QR + (head * 64 + T) * 128;
            *((LAS u32x2*)(lds + Q_TAB) + lw * 64 + lane) = (u32x2){qm == 4 ? er : em, qm == 4 ? (isk ? BLOB : 8192) : 0u};
            WAITL();
        }
        int slot = 0;
        ROLL for (int t = 1; t <= 5; t++) { waitv<16>(); BAR(); issue(t + 3, slot); slot = (slot + 1) & 3; }
        UNR for (int k = 0; k < 3; k++) {
            const unsigned il = lw * 64 + lane + 256 * k, i = ((mt & 1) * 14 + nt) * 672 + il; st128((u32x4){~0u, ~0u, ~0u, ~0u}, sw, il < 672 && i < 18432 ? i * 16 : OOB, WS_OF + (mt >> 1) * 294912); }
        waitv<19>(); BAR(); waitv<11>(); BAR(); waitv<3>(); BAR();
        BAR();
        if (nt >= 9 && lw < 2) *(LAS u32x4*)((LAS char*)lds + (lw * 64 + lane) * 304 + 288) = (u32x4){ONES, ONES, ONES, ONES};
    }
    WAITL(); BAR();
    if (nt >= 9) {
        const int col0 = nt * 128 - 2 * D, head = ((col0 * 3641) >> 16) + wid, d0 = lane % 19, lo = col0 - 18 * head, hi = head < 32 ? lo + 128 : 0;
        u32x4 v[5];
        UNR for (int k = 0; k < 5; k++) v[k] = *(LAS const u32x4*)((LAS const char*)lds + wid * 4864 + lane * 16 + k * 1024);
        UNR for (int k = 0; k < 5; k++) { int d = d0 + (7 * k) % 19; d = d >= 19 ? d - 19 : d; d = d == 18 ? 17 : d;
            st128(v[k], sw, d >= lo && d < hi && (k < 4 || lane < 48) ? lane * 16 + k * 1024 : OOB, WS_KV + head * BLOB + (mt >> 1) * CHUNK + 9216 + (mt & 1) * 4864); }
    } else {
        const int c16 = tid & 15; u32x2 e[4]; u32x4 v[4], b[4];
        UNR for (int k = 0; k < 4; k++) e[k] = *((LAS const u32x2*)(lds + Q_TAB) + (tid >> 4) + 32 * k);
        UNR for (int k = 0; k < 4; k++) { LAS const char* a = (LAS const char*)lds + ((tid >> 4) + 32 * k) * 256 + (e[k][1] ? (c16 & 7) * 32 : c16 * 16); v[k] = *(LAS const u32x4*)a; b[k] = *(LAS const u32x4*)(a + 16); }
        const bool split = e[0][1] != 0, up = c16 >= 8;
        const unsigned dl = c16 * 16 + (split && up ? e[0][1] - 128 : 0);
        if (__builtin_amdgcn_ballot_w64(split)) UNR for (int k = 0; k < 4; k++) if (split) v[k] = up ? (u32x4){v[k][1], v[k][3], b[k][1], b[k][3]} : (u32x4){v[k][0], v[k][2], b[k][0], b[k][2]};
        UNR for (int k = 0; k < 4; k++) st128(v[k], sw, e[k][0] + dl, 0);
    }
}

constexpr int O_X = 9 * 12288, O_T = O_X + 12288, O_E = O_T + 4 * 3328;
DI unsigned phase_attn(char* lds, char* ws, const bf16_t* w1, const bf16_t* w2, srd_t sw, unsigned tl, unsigned th, int bid, int wid, int lane) {
    const int xg = bid & 7, ix = bid >> 3, head = xg * 4 + (ix & 3), qp = bid >> 5, T = qp * 8 + wid, r = lane & 31, h = lane >> 5;
    const unsigned lds0 = (unsigned)(uintptr_t)lds, v16 = lane * 16;
    const srd_t skv = mksrd(ws + WS_KV + (size_t)head * BLOB, BLOB);
    const unsigned ka = h * 256 + r * 8, kra = 8192 + r * 4, va = 9216 + h * 304 + r * 16;
    const srd_t spf = mksrd(wid < 2 ? (const void*)w2 : wid < 4 ? (const void*)w1 : (const void*)(ws + WS_WP), wid < 4 ? S * F * 2 : D * D * 2);
    const unsigned pfs = wid < 2 ? 4 * (S * 2) : wid < 4 ? 128 : 8192; unsigned pfn = wid < 4 ? 7 : 0;
    const unsigned pfv = wid < 2 ? ((xg >> 1) * 1152 + 36 * ix + 16 * wid + (lane >> 4)) * (S * 2) + (xg & 1) * 2048 + (lane & 15) * 128
                       : wid < 4 ? (64 * ix + lane) * (F * 2) + ((9 * xg) >> 1) * 256 + (wid - 2) * 640 : (ix * 162 + lane) * 128;
    if (wid == 4) {
        const int part = lane >> 4, mt = lane & 15, nt = ((1 + (part >> 1)) * D + head * 18 + (part & 1) * 17) >> 7;
        const int nq = (lane >> 1) % 14, mq = 2 * (lane < 28 ? qp : xg) + (lane & 1);
        unsigned o1 = ((nt % 7 * 4 + (mt & 3)) * 8 + (mt >> 2) * 2 + nt / 7) * 128;
        unsigned o2 = lane < 56 ? ((nq % 7 * 4 + (mq & 3)) * 8 + (mq >> 2) * 2 + nq / 7) * 128 : OOB, o3 = lane < 32 ? (224 + lane) * 128 : OOB;
        ROLL for (unsigned spins = 0; spins < SPIN_CAP; spins++) {
            asm volatile("" ::: "memory");
            const u32x2 a = ld64(sw, o1, WS_F1 + xg * 32768), b = ld64(sw, o2, WS_F1 + xg * 32768), c = ld64(sw, o3, WS_F1 + xg * 32768);
            if (a[0] == tl && a[1] == th) o1 = OOB;
            if (b[0] == tl && b[1] == th) o2 = OOB;
            if (c[0] == tl && c[1] == th) o3 = OOB;
            if (__all((o1 & o2 & o3) == OOB)) break;
            __builtin_amdgcn_s_sleep(2);
        }
    }
    BAR();
    UNR for (int i = 0; i < 3; i++) dma_dev(lds0 + (wid + 8 * i) * 1024, v16, skv, (wid + 8 * i) * 1024);
    const u32x2 q0 = ld64(sw, ka, WS_QB + (head * 64 + T) * 1024), q1 = ld64(sw, ka + 512, WS_QB + (head * 64 + T) * 1024);
    const unsigned q2 = ld32(sw, r * 4, WS_QR + (head * 64 + T) * 128);
    const bf16x8 qm = __builtin_bit_cast(bf16x8, (u32x4){q0[0], q0[1], q1[0], q1[1]});
    u32x2 fw = {~tl, 0u}; if (wid == 5) fw = image_flags(sw, lane);
    waitv<0>(); BAR();
    if (wid == 5 && __all(fw[0] == tl && fw[1] == th)) pfn = 5;
    const unsigned so = WS_OF + T * 36864, vo = (r >> 4) * 18432 + (r & 15) * 16;
    const unsigned om = so + (head >> 1) * 1024 + (head & 1) * 512, orr = so + (16 + (head >> 4)) * 1024 + ((head & 15) >> 2) * 256 + (head & 3) * 4;
    const unsigned opx = WS_PX + (((((xg & 1) * 8 + (ix >> 2)) * 4 + (ix & 3)) * 4 + (wid >> 1)) * 12 + (xg >> 1) * 3) * 2560 + (wid & 1) * 3840;
    const unsigned vm = vo + h * 256, vr = h ? OOB : vo, vl = lane < 48 ? v16 : OOB;
    f32x16 O; unsigned early;
    const unsigned vf = bid < 192 ? r * 128 : OOB, wp = WS_WP + (ix % 6) * O_X + wid * 1024;
    {
        unsigned a0 = lds0 + ka, a1 = lds0 + 8192 + lane * 4, a2 = lds0 + va;
        asm volatile(
            "s_mov_b32 s36, 0x7ffff\ns_mov_b32 s37, 0x7ffff\ns_mov_b32 s38, 2\ns_mov_b32 s41, 0\ns_mov_b32 s39, %[p0]\ns_add_u32 s40, %[p0], %[lds]\ns_mov_b32 %[early], 0\ns_cmp_ge_u32 %[wid], 4\ns_cselect_b64 vcc, -1, 0\ns_setprio 1\ns_cbranch_vccnz 21f\ns_setprio 2\n21:\n"
            "ds_read_b32 v60, v77\nds_read2st64_b64 v[56:59], v76 offset0:0 offset1:1\nds_read2st64_b64 v[90:93], v76 offset0:2 offset1:3\nv_mov_b32 v61, 0x3f80\nv_mov_b32 v75, 0\n"
            "v_mov_b32 v62, 0\nv_mov_b32 v63, 0\nv_mov_b32 v64, 0\nv_mov_b32 v65, 0\nv_mov_b32 v66, 0\nv_mov_b32 v67, 0\nv_mov_b32 v68, 0\nv_mov_b32 v69, 0\n"
            "s_waitcnt lgkmcnt(2)\nv_mfma_f32_32x32x4_2b_bf16 v[0:31], v[60:61], v[74:75], 0\nds_read_b32 v60, v77 offset:256\n"
            "v_mov_b32 v32, 0\nv_mov_b32 v33, 0\nv_mov_b32 v34, 0\nv_mov_b32 v35, 0\nv_mov_b32 v36, 0\nv_mov_b32 v37, 0\nv_mov_b32 v38, 0\nv_mov_b32 v39, 0\nv_mov_b32 v40, 0\nv_mov_b32 v41, 0\nv_mov_b32 v42, 0\nv_mov_b32 v43, 0\nv_mov_b32 v44, 0\nv_mov_b32 v45, 0\nv_mov_b32 v46, 0\nv_mov_b32 v47, 0\nv_mov_b32 v52, 0\nv_mov_b32 v53, 0\nv_mov_b32 v54, 0\nv_mov_b32 v55, 0\n"
            "s_waitcnt lgkmcnt(1)\nv_mfma_f32_32x32x16_bf16 v[0:15], v[56:59], v[70:73], v[0:15]\nv_mfma_f32_32x32x16_bf16 v[16:31], v[90:93], v[70:73], v[16:31]\nds_read2st64_b64 v[56:59], v76 offset0:4 offset1:5\nds_read2st64_b64 v[90:93], v76 offset0:6 offset1:7\ns_mov_b64 exec, s[36:37]\nds_read_b128 v[66:69], v78 offset:608\nds_read_b128 v[62:65], v78\ns_mov_b64 exec, -1\ns_nop 5\n"
            "v_max3_f32 v48, v0, v1, v2\nv_max3_f32 v49, v3, v4, v5\nv_max3_f32 v50, v6, v7, v8\nv_max3_f32 v51, v9, v10, v11\nv_max3_f32 v48, v48, v12, v13\nv_max3_f32 v49, v49, v14, v15\nv_max3_f32 v48, v48, v49, v50\nv_max_f32 v48, v48, v51\nv_mov_b32 v49, v48\ns_nop 1\nv_permlane32_swap_b32 v48, v49\nv_max_f32 v48, v48, v49\nv_cvt_pk_bf16_f32 v75, -v48, 0\nv_lshlrev_b32 v48, 16, v75\n"
            "v_pk_add_f32 v[0:1], v[0:1], v[48:49] op_sel_hi:[1,0]\nv_pk_add_f32 v[2:3], v[2:3], v[48:49] op_sel_hi:[1,0]\nv_pk_add_f32 v[4:5], v[4:5], v[48:49] op_sel_hi:[1,0]\nv_pk_add_f32 v[6:7], v[6:7], v[48:49] op_sel_hi:[1,0]\nv_pk_add_f32 v[8:9], v[8:9], v[48:49] op_sel_hi:[1,0]\nv_pk_add_f32 v[10:11], v[10:11], v[48:49] op_sel_hi:[1,0]\nv_pk_add_f32 v[12:13], v[12:13], v[48:49] op_sel_hi:[1,0]\nv_pk_add_f32 v[14:15], v[14:15], v[48:49] op_sel_hi:[1,0]\nv_pk_add_f32 v[16:17], v[16:17], v[48:49] op_sel_hi:[1,0]\nv_pk_add_f32 v[18:19], v[18:19], v[48:49] op_sel_hi:[1,0]\nv_pk_add_f32 v[20:21], v[20:21], v[48:49] op_sel_hi:[1,0]\nv_pk_add_f32 v[22:23], v[22:23], v[48:49] op_sel_hi:[1,0]\nv_pk_add_f32 v[24:25], v[24:25], v[48:49] op_sel_hi:[1,0]\nv_pk_add_f32 v[26:27], v[26:27], v[48:49] op_sel_hi:[1,0]\nv_pk_add_f32 v[28:29], v[28:29], v[48:49] op_sel_hi:[1,0]\nv_pk_add_f32 v[30:31], v[30:31], v[48:49] op_sel_hi:[1,0]\ns_waitcnt lgkmcnt(0)\n.p2align 6\n"
            "v_mfma_f32_32x32x4_2b_bf16 v[96:127], v[60:61], v[74:75], 0\nds_read_b32 v60, v77 offset:512\n"
            "v_exp_f32 v0, v0\nv_exp_f32 v1, v1\nv_exp_f32 v2, v2\nv_exp_f32 v3, v3\nv_cvt_pk_bf16_f32 v48, v0, v1\nv_cvt_pk_bf16_f32 v49, v2, v3\ns_mov_b32 m0, s40\ns_add_u32 s40, s40, 0x2000\nbuffer_load_dwordx4 v79, %[kv], s39 offen sc1 lds\ns_add_u32 s39, s39, 0x2000\n"
            "v_exp_f32 v4, v4\nv_exp_f32 v5, v5\nv_exp_f32 v6, v6\nv_exp_f32 v7, v7\nv_cvt_pk_bf16_f32 v50, v4, v5\nv_cvt_pk_bf16_f32 v51, v6, v7\n"
            "s_waitcnt lgkmcnt(1)\nv_mfma_f32_32x32x16_bf16 v[32:47], v[66:69], v[52:55], v[32:47]\ns_mov_b32 m0, s40\ns_add_u32 s40, s40, 0x2000\nbuffer_load_dwordx4 v79, %[kv], s39 offen sc1 lds\ns_add_u32 s39, s39, 0x2000\n"
            "v_exp_f32 v8, v8\nv_exp_f32 v9, v9\nv_exp_f32 v10, v10\nv_exp_f32 v11, v11\nv_cvt_pk_bf16_f32 v52, v8, v9\nv_cvt_pk_bf16_f32 v53, v10, v11\n"
            "v_mfma_f32_32x32x16_bf16 v[32:47], v[62:65], v[48:51], v[32:47]\ns_mov_b64 exec, s[36:37]\nds_read_b128 v[66:69], v78 offset:608\nds_read_b128 v[62:65], v78 offset:1216\ns_mov_b64 exec, -1\n"
            "v_exp_f32 v12, v12\nv_exp_f32 v13, v13\nv_exp_f32 v14, v14\nv_exp_f32 v15, v15\nv_cvt_pk_bf16_f32 v54, v12, v13\nv_cvt_pk_bf16_f32 v55, v14, v15\n"
            "v_mfma_f32_32x32x16_bf16 v[96:111], v[56:59], v[70:73], v[96:111]\nds_read2st64_b64 v[56:59], v76 offset0:8 offset1:9\n"
            "v_exp_f32 v16, v16\nv_exp_f32 v17, v17\nv_exp_f32 v18, v18\nv_exp_f32 v19, v19\nv_cvt_pk_bf16_f32 v48, v16, v17\nv_cvt_pk_bf16_f32 v49, v18, v19\n"
            "v_mfma_f32_32x32x16_bf16 v[112:127], v[90:93], v[70:73], v[112:127]\nds_read2st64_b64 v[90:93], v76 offset0:10 offset1:11\ns_mov_b32 m0, s40\ns_add_u32 s40, s40, 0x2000\nbuffer_load_dwordx4 v79, %[kv], s39 offen sc1 lds\ns_add_u32 s39, s39, 0x2000\n"
            "v_exp_f32 v20, v20\nv_exp_f32 v21, v21\nv_exp_f32 v22, v22\nv_exp_f32 v23, v23\nv_cvt_pk_bf16_f32 v50, v20, v21\nv_cvt_pk_bf16_f32 v51, v22, v23\n"
            "s_waitcnt lgkmcnt(2)\nv_mfma_f32_32x32x16_bf16 v[32:47], v[66:69], v[52:55], v[32:47]\n"
            "v_exp_f32 v24, v24\nv_exp_f32 v25, v25\nv_exp_f32 v26, v26\nv_exp_f32 v27, v27\nv_cvt_pk_bf16_f32 v52, v24, v25\nv_cvt_pk_bf16_f32 v53, v26, v27\n"
            "v_mfma_f32_32x32x16_bf16 v[32:47], v[62:65], v[48:51], v[32:47]\ns_mov_b64 exec, s[36:37]\nds_read_b128 v[66:69], v78 offset:1824\nds_read_b128 v[62:65], v78 offset:2432\ns_mov_b64 exec, -1\n"
            "v_exp_f32 v28, v28\nv_exp_f32 v29, v29\nv_exp_f32 v30, v30\nv_exp_f32 v31, v31\nv_cvt_pk_bf16_f32 v54, v28, v29\nv_cvt_pk_bf16_f32 v55, v30, v31\n"
            "v_mfma_f32_32x32x4_2b_bf16 v[0:31], v[60:61], v[74:75], 0\nds_read_b32 v60, v77 offset:768\n"
            "v_exp_f32 v96, v96\nv_exp_f32 v97, v97\nv_exp_f32 v98, v98\nv_exp_f32 v99, v99\nv_cvt_pk_bf16_f32 v48, v96, v97\nv_cvt_pk_bf16_f32 v49, v98, v99\ns_mov_b32 m0, s40\ns_add_u32 s40, s40, 0x2000\nbuffer_load_dwordx4 v79, %[kv], s39 offen sc1 lds\ns_add_u32 s39, s39, 0x2000\n"
            "v_exp_f32 v100, v100\nv_exp_f32 v101, v101\nv_exp_f32 v102, v102\nv_exp_f32 v103, v103\nv_cvt_pk_bf16_f32 v50, v100, v101\nv_cvt_pk_bf16_f32 v51, v102, v103\n"
            "s_waitcnt lgkmcnt(1)\nv_mfma_f32_32x32x16_bf16 v[32:47], v[66:69], v[52:55], v[32:47]\n"
            "v_exp_f32 v104, v104\nv_exp_f32 v105, v105\nv_exp_f32 v106, v106\nv_exp_f32 v107, v107\nv_cvt_pk_bf16_f32 v52, v104, v105\nv_cvt_pk_bf16_f32 v53, v106, v107\n"
            "v_mfma_f32_32x32x16_bf16 v[32:47], v[62:65], v[48:51], v[32:47]\ns_mov_b64 exec, s[36:37]\nds_read_b128 v[66:69], v78 offset:3040\nds_read_b128 v[62:65], v78 offset:3648\ns_mov_b64 exec, -1\n"
            "v_exp_f32 v108, v108\nv_exp_f32 v109, v109\nv_exp_f32 v110, v110\nv_exp_f32 v111, v111\nv_cvt_pk_bf16_f32 v54, v108, v109\nv_cvt_pk_bf16_f32 v55, v110, v111\n"
            "s_cbranch_vccnz 22f\ns_setprio 0\n22:\n"
            "v_mfma_f32_32x32x16_bf16 v[0:15], v[56:59], v[70:73], v[0:15]\nds_read2st64_b64 v[56:59], v76 offset0:12 offset1:13\n"
            "v_exp_f32 v112, v112\nv_exp_f32 v113, v113\nv_exp_f32 v114, v114\nv_exp_f32 v115, v115\nv_cvt_pk_bf16_f32 v48, v112, v113\nv_cvt_pk_bf16_f32 v49, v114, v115\n"
            "v_mfma_f32_32x32x16_bf16 v[16:31], v[90:93], v[70:73], v[16:31]\nds_read2st64_b64 v[90:93], v76 offset0:14 offset1:15\ns_mov_b32 m0, s40\ns_add_u32 s40, s40, 0x2000\nbuffer_load_dwordx4 v79, %[kv], s39 offen sc1 lds\ns_add_u32 s39, s39, 0x2000\n"
            "v_exp_f32 v116, v116\nv_exp_f32 v117, v117\nv_exp_f32 v118, v118\nv_exp_f32 v119, v119\nv_cvt_pk_bf16_f32 v50, v116, v117\nv_cvt_pk_bf16_f32 v51, v118, v119\n"
            "s_waitcnt lgkmcnt(2)\nv_mfma_f32_32x32x16_bf16 v[32:47], v[66:69], v[52:55], v[32:47]\n"
            "v_exp_f32 v120, v120\nv_exp_f32 v121, v121\nv_exp_f32 v122, v122\nv_exp_f32 v123, v123\nv_cvt_pk_bf16_f32 v52, v120, v121\nv_cvt_pk_bf16_f32 v53, v122, v123\n"
            "v_mfma_f32_32x32x16_bf16 v[32:47], v[62:65], v[48:51], v[32:47]\ns_mov_b64 exec, s[36:37]\nds_read_b128 v[66:69], v78 offset:4256\nds_read_b128 v[62:65], v78 offset:4864\ns_mov_b64 exec, -1\n"
            "v_exp_f32 v124, v124\nv_exp_f32 v125, v125\nv_exp_f32 v126, v126\nv_exp_f32 v127, v127\nv_cvt_pk_bf16_f32 v54, v124, v125\nv_cvt_pk_bf16_f32 v55, v126, v127\n"
            "s_waitcnt vmcnt(3)\ns_barrier\ns_cbranch_vccnz 23f\ns_setprio 2\n23:\n"
            "v_mfma_f32_32x32x4_2b_bf16 v[96:127], v[60:61], v[74:75], 0\nds_read_b32 v60, v77 offset:18944\n"
            "v_exp_f32 v0, v0\nv_exp_f32 v1, v1\nv_exp_f32 v2, v2\nv_exp_f32 v3, v3\nv_cvt_pk_bf16_f32 v48, v0, v1\nv_cvt_pk_bf16_f32 v49, v2, v3\ns_mov_b32 m0, s40\ns_add_u32 s40, s40, 0x2000\nbuffer_load_dwordx4 v79, %[kv], s39 offen sc1 lds\ns_add_u32 s39, s39, 0x2000\n"
            "v_exp_f32 v4, v4\nv_exp_f32 v5, v5\nv_exp_f32 v6, v6\nv_exp_f32 v7, v7\nv_cvt_pk_bf16_f32 v50, v4, v5\nv_cvt_pk_bf16_f32 v51, v6, v7\n"
            "s_waitcnt lgkmcnt(1)\nv_mfma_f32_32x32x16_bf16 v[32:47], v[66:69], v[52:55], v[32:47]\ns_mov_b32 m0, s40\ns_add_u32 s40, s40, 0x2000\nbuffer_load_dwordx4 v79, %[kv], s39 offen sc1 lds\ns_add_u32 s39, s39, 0x2000\n"
            "v_exp_f32 v8, v8\nv_exp_f32 v9, v9\nv_exp_f32 v10, v10\nv_exp_f32 v11, v11\nv_cvt_pk_bf16_f32 v52, v8, v9\nv_cvt_pk_bf16_f32 v53, v10, v11\n"
            "v_mfma_f32_32x32x16_bf16 v[32:47], v[62:65], v[48:51], v[32:47]\ns_mov_b64 exec, s[36:37]\nds_read_b128 v[66:69], v78 offset:5472\nds_read_b128 v[62:65], v78 offset:6080\ns_mov_b64 exec, -1\n"
            "v_exp_f32 v12, v12\nv_exp_f32 v13, v13\nv_exp_f32 v14, v14\nv_exp_f32 v15, v15\nv_cvt_pk_bf16_f32 v54, v12, v13\nv_cvt_pk_bf16_f32 v55, v14, v15\n"
            "v_mfma_f32_32x32x16_bf16 v[96:111], v[56:59], v[70:73], v[96:111]\nds_read2st64_b64 v[56:59], v76 offset0:37 offset1:38\n"
            "v_exp_f32 v16, v16\nv_exp_f32 v17, v17\nv_exp_f32 v18, v18\nv_exp_f32 v19, v19\nv_cvt_pk_bf16_f32 v48, v16, v17\nv_cvt_pk_bf16_f32 v49, v18, v19\n"
            "v_mfma_f32_32x32x16_bf16 v[112:127], v[90:93], v[70:73], v[112:127]\nds_read2st64_b64 v[90:93], v76 offset0:39 offset1:40\ns_mov_b32 m0, s40\ns_add_u32 s40, s40, 0x2000\nbuffer_load_dwordx4 v79, %[kv], s39 offen sc1 lds\ns_add_u32 s39, s39, 0x2000\n"
            "v_exp_f32 v20, v20\nv_exp_f32 v21, v21\nv_exp_f32 v22, v22\nv_exp_f32 v23, v23\nv_cvt_pk_bf16_f32 v50, v20, v21\nv_cvt_pk_bf16_f32 v51, v22, v23\n"
            "s_waitcnt lgkmcnt(2)\nv_mfma_f32_32x32x16_bf16 v[32:47], v[66:69], v[52:55], v[32:47]\ns_mov_b32 m0, s40\ns_add_u32 s40, s40, 0x2000\nbuffer_load_dwordx4 v79, %[kv], s39 offen sc1 lds\ns_add_u32 s39, s39, 0x2000\n"
            "v_exp_f32 v24, v24\nv_exp_f32 v25, v25\nv_exp_f32 v26, v26\nv_exp_f32 v27, v27\nv_cvt_pk_bf16_f32 v52, v24, v25\nv_cvt_pk_bf16_f32 v53, v26, v27\n"
            "v_mfma_f32_32x32x16_bf16 v[32:47], v[62:65], v[48:51], v[32:47]\ns_mov_b64 exec, s[36:37]\nds_read_b128 v[66:69], v78 offset:6688\nds_read_b128 v[62:65], v78 offset:7296\ns_mov_b64 exec, -1\n"
            "v_exp_f32 v28, v28\nv_exp_f32 v29, v29\nv_exp_f32 v30, v30\nv_exp_f32 v31, v31\nv_cvt_pk_bf16_f32 v54, v28, v29\nv_cvt_pk_bf16_f32 v55, v30, v31\n"
            "v_mfma_f32_32x32x4_2b_bf16 v[0:31], v[60:61], v[74:75], 0\nds_read_b32 v60, v77 offset:19200\n"
            "v_exp_f32 v96, v96\nv_exp_f32 v97, v97\nv_exp_f32 v98, v98\nv_exp_f32 v99, v99\nv_cvt_pk_bf16_f32 v48, v96, v97\nv_cvt_pk_bf16_f32 v49, v98, v99\ns_mov_b32 m0, s40\ns_add_u32 s40, s40, 0x2000\nbuffer_load_dwordx4 v79, %[kv], s39 offen sc1 lds\ns_add_u32 s39, s39, 0x2000\n"
            "v_exp_f32 v100, v100\nv_exp_f32 v101, v101\nv_exp_f32 v102, v102\nv_exp_f32 v103, v103\nv_cvt_pk_bf16_f32 v50, v100, v101\nv_cvt_pk_bf16_f32 v51, v102, v103\n"
            "s_waitcnt lgkmcnt(1)\nv_mfma_f32_32x32x16_bf16 v[32:47], v[66:69], v[52:55], v[32:47]\ns_mov_b32 m0, s40\ns_add_u32 s40, s40, 0x2000\nbuffer_load_dwordx4 v79, %[kv], s39 offen sc1 lds\ns_add_u32 s39, s39, 0x2000\n"
            "v_exp_f32 v104, v104\nv_exp_f32 v105, v105\nv_exp_f32 v106, v106\nv_exp_f32 v107, v107\nv_cvt_pk_bf16_f32 v52, v104, v105\nv_cvt_pk_bf16_f32 v53, v106, v107\n"
            "v_mfma_f32_32x32x16_bf16 v[32:47], v[62:65], v[48:51], v[32:47]\ns_mov_b64 exec, s[36:37]\nds_read_b128 v[66:69], v78 offset:7904\nds_read_b128 v[62:65], v78 offset:8512\ns_mov_b64 exec, -1\n"
            "v_exp_f32 v108, v108\nv_exp_f32 v109, v109\nv_exp_f32 v110, v110\nv_exp_f32 v111, v111\nv_cvt_pk_bf16_f32 v54, v108, v109\nv_cvt_pk_bf16_f32 v55, v110, v111\n"
            "v_mfma_f32_32x32x16_bf16 v[0:15], v[56:59], v[70:73], v[0:15]\nds_read2st64_b64 v[56:59], v76 offset0:41 offset1:42\n"
            "v_exp_f32 v112, v112\nv_exp_f32 v113, v113\nv_exp_f32 v114, v114\nv_exp_f32 v115, v115\nv_cvt_pk_bf16_f32 v48, v112, v113\nv_cvt_pk_bf16_f32 v49, v114, v115\n"
            "v_mfma_f32_32x32x16_bf16 v[16:31], v[90:93], v[70:73], v[16:31]\nds_read2st64_b64 v[90:93], v76 offset0:43 offset1:44\ns_mov_b32 m0, s40\ns_add_u32 s40, s40, 0x2000\nbuffer_load_dwordx4 v79, %[kv], s39 offen sc1 lds\ns_add_u32 s39, s39, 0x2000\n"
            "v_exp_f32 v116, v116\nv_exp_f32 v117, v117\nv_exp_f32 v118, v118\nv_exp_f32 v119, v119\nv_cvt_pk_bf16_f32 v50, v116, v117\nv_cvt_pk_bf16_f32 v51, v118, v119\n"
            "s_waitcnt lgkmcnt(2)\nv_mfma_f32_32x32x16_bf16 v[32:47], v[66:69], v[52:55], v[32:47]\ns_mov_b32 m0, s40\ns_add_u32 s40, s40, 0x2000\nbuffer_load_dwordx4 v79, %[kv], s39 offen sc1 lds\ns_add_u32 s39, s39, 0x2000\n"
            "v_exp_f32 v120, v120\nv_exp_f32 v121, v121\nv_exp_f32 v122, v122\nv_exp_f32 v123, v123\nv_cvt_pk_bf16_f32 v52, v120, v121\nv_cvt_pk_bf16_f32 v53, v122, v123\n"
            "v_mfma_f32_32x32x16_bf16 v[32:47], v[62:65], v[48:51], v[32:47]\ns_mov_b64 exec, s[36:37]\nds_read_b128 v[66:69], v78 offset:9120\nds_read_b128 v[62:65], v78 offset:18944\ns_mov_b64 exec, -1\n"
            "v_exp_f32 v124, v124\nv_exp_f32 v125, v125\nv_exp_f32 v126, v126\nv_exp_f32 v127, v127\nv_cvt_pk_bf16_f32 v54, v124, v125\nv_cvt_pk_bf16_f32 v55, v126, v127\n"
            "v_mfma_f32_32x32x4_2b_bf16 v[96:127], v[60:61], v[74:75], 0\nds_read_b32 v60, v77 offset:19456\n"
            "v_exp_f32 v0, v0\nv_exp_f32 v1, v1\nv_exp_f32 v2, v2\nv_exp_f32 v3, v3\nv_cvt_pk_bf16_f32 v48, v0, v1\nv_cvt_pk_bf16_f32 v49, v2, v3\ns_mov_b32 m0, s40\ns_add_u32 s40, s40, 0x2000\nbuffer_load_dwordx4 v79, %[kv], s39 offen sc1 lds\ns_add_u32 s39, s39, 0x2000\n"
            "v_exp_f32 v4, v4\nv_exp_f32 v5, v5\nv_exp_f32 v6, v6\nv_exp_f32 v7, v7\nv_cvt_pk_bf16_f32 v50, v4, v5\nv_cvt_pk_bf16_f32 v51, v6, v7\n"
            "s_waitcnt lgkmcnt(1)\nv_mfma_f32_32x32x16_bf16 v[32:47], v[66:69], v[52:55], v[32:47]\ns_mov_b32 m0, s40\ns_add_u32 s40, s40, 0x2000\nbuffer_load_dwordx4 v79, %[kv], s39 offen sc1 lds\ns_add_u32 s39, s39, 0x2000\n"
            "v_exp_f32 v8, v8\nv_exp_f32 v9, v9\nv_exp_f32 v10, v10\nv_exp_f32 v11, v11\nv_cvt_pk_bf16_f32 v52, v8, v9\nv_cvt_pk_bf16_f32 v53, v10, v11\n"
            "v_mfma_f32_32x32x16_bf16 v[32:47], v[62:65], v[48:51], v[32:47]\ns_mov_b64 exec, s[36:37]\nds_read_b128 v[66:69], v78 offset:19552\nds_read_b128 v[62:65], v78 offset:20160\ns_mov_b64 exec, -1\n"
            "v_exp_f32 v12, v12\nv_exp_f32 v13, v13\nv_exp_f32 v14, v14\nv_exp_f32 v15, v15\nv_cvt_pk_bf16_f32 v54, v12, v13\nv_cvt_pk_bf16_f32 v55, v14, v15\n"
            "v_mfma_f32_32x32x16_bf16 v[96:111], v[56:59], v[70:73], v[96:111]\nds_read2st64_b64 v[56:59], v76 offset0:45 offset1:46\n"
            "v_exp_f32 v16, v16\nv_exp_f32 v17, v17\nv_exp_f32 v18, v18\nv_exp_f32 v19, v19\nv_cvt_pk_bf16_f32 v48, v16, v17\nv_cvt_pk_bf16_f32 v49, v18, v19\n"
            "v_mfma_f32_32x32x16_bf16 v[112:127], v[90:93], v[70:73], v[112:127]\nds_read2st64_b64 v[90:93], v76 offset0:47 offset1:48\ns_mov_b32 m0, s40\ns_add_u32 s40, s40, 0x2000\nbuffer_load_dwordx4 v79, %[kv], s39 offen sc1 lds\ns_add_u32 s39, s39, 0x2000\n"
            "v_exp_f32 v20, v20\nv_exp_f32 v21, v21\nv_exp_f32 v22, v22\nv_exp_f32 v23, v23\nv_cvt_pk_bf16_f32 v50, v20, v21\nv_cvt_pk_bf16_f32 v51, v22, v23\n"
            "s_waitcnt lgkmcnt(2)\nv_mfma_f32_32x32x16_bf16 v[32:47], v[66:69], v[52:55], v[32:47]\n"
            "v_exp_f32 v24, v24\nv_exp_f32 v25, v25\nv_exp_f32 v26, v26\nv_exp_f32 v27, v27\nv_cvt_pk_bf16_f32 v52, v24, v25\nv_cvt_pk_bf16_f32 v53, v26, v27\n"
            "v_mfma_f32_32x32x16_bf16 v[32:47], v[62:65], v[48:51], v[32:47]\ns_mov_b64 exec, s[36:37]\nds_read_b128 v[66:69], v78 offset:20768\nds_read_b128 v[62:65], v78 offset:21376\ns_mov_b64 exec, -1\n"
            "v_exp_f32 v28, v28\nv_exp_f32 v29, v29\nv_exp_f32 v30, v30\nv_exp_f32 v31, v31\nv_cvt_pk_bf16_f32 v54, v28, v29\nv_cvt_pk_bf16_f32 v55, v30, v31\n"
            "v_mfma_f32_32x32x4_2b_bf16 v[0:31], v[60:61], v[74:75], 0\nds_read_b32 v60, v77 offset:19712\n"
            "v_exp_f32 v96, v96\nv_exp_f32 v97, v97\nv_exp_f32 v98, v98\nv_exp_f32 v99, v99\nv_cvt_pk_bf16_f32 v48, v96, v97\nv_cvt_pk_bf16_f32 v49, v98, v99\n"
            "v_exp_f32 v100, v100\nv_exp_f32 v101, v101\nv_exp_f32 v102, v102\nv_exp_f32 v103, v103\nv_cvt_pk_bf16_f32 v50, v100, v101\nv_cvt_pk_bf16_f32 v51, v102, v103\n"
            "s_waitcnt lgkmcnt(1)\nv_mfma_f32_32x32x16_bf16 v[32:47], v[66:69], v[52:55], v[32:47]\n"
            "v_exp_f32 v104, v104\nv_exp_f32 v105, v105\nv_exp_f32 v106, v106\nv_exp_f32 v107, v107\nv_cvt_pk_bf16_f32 v52, v104, v105\nv_cvt_pk_bf16_f32 v53, v106, v107\n"
            "v_mfma_f32_32x32x16_bf16 v[32:47], v[62:65], v[48:51], v[32:47]\ns_mov_b64 exec, s[36:37]\nds_read_b128 v[66:69], v78 offset:21984\nds_read_b128 v[62:65], v78 offset:22592\ns_mov_b64 exec, -1\n"
            "v_exp_f32 v108, v108\nv_exp_f32 v109, v109\nv_exp_f32 v110, v110\nv_exp_f32 v111, v111\nv_cvt_pk_bf16_f32 v54, v108, v109\nv_cvt_pk_bf16_f32 v55, v110, v111\n"
            "s_cbranch_vccnz 24f\ns_setprio 0\n24:\n"
            "v_mfma_f32_32x32x16_bf16 v[0:15], v[56:59], v[70:73], v[0:15]\nds_read2st64_b64 v[56:59], v76 offset0:49 offset1:50\n"
            "v_exp_f32 v112, v112\nv_exp_f32 v113, v113\nv_exp_f32 v114, v114\nv_exp_f32 v115, v115\nv_cvt_pk_bf16_f32 v48, v112, v113\nv_cvt_pk_bf16_f32 v49, v114, v115\n"
            "v_mfma_f32_32x32x16_bf16 v[16:31], v[90:93], v[70:73], v[16:31]\nds_read2st64_b64 v[90:93], v76 offset0:51 offset1:52\n"
            "v_exp_f32 v116, v116\nv_exp_f32 v117, v117\nv_exp_f32 v118, v118\nv_exp_f32 v119, v119\nv_cvt_pk_bf16_f32 v50, v116, v117\nv_cvt_pk_bf16_f32 v51, v118, v119\n"
            "s_waitcnt lgkmcnt(2)\nv_mfma_f32_32x32x16_bf16 v[32:47], v[66:69], v[52:55], v[32:47]\n"
            "v_exp_f32 v120, v120\nv_exp_f32 v121, v121\nv_exp_f32 v122, v122\nv_exp_f32 v123, v123\nv_cvt_pk_bf16_f32 v52, v120, v121\nv_cvt_pk_bf16_f32 v53, v122, v123\n"
            "v_mfma_f32_32x32x16_bf16 v[32:47], v[62:65], v[48:51], v[32:47]\ns_mov_b64 exec, s[36:37]\nds_read_b128 v[66:69], v78 offset:23200\nds_read_b128 v[62:65], v78 offset:23808\ns_mov_b64 exec, -1\n"
            "v_exp_f32 v124, v124\nv_exp_f32 v125, v125\nv_exp_f32 v126, v126\nv_exp_f32 v127, v127\nv_cvt_pk_bf16_f32 v54, v124, v125\nv_cvt_pk_bf16_f32 v55, v126, v127\n"
            "s_waitcnt vmcnt(0)\ns_barrier\ns_cbranch_vccnz 25f\ns_setprio 2\n25:\nv_mov_b32 v82, -1\nv_mov_b32 v83, -1\nv_mov_b32 v84, -1\nv_mov_b32 v85, -1\nbuffer_store_dwordx4 v[82:85], v79, %[ws], %[opx] offen offset:0 sc1\nbuffer_store_dwordx4 v[82:85], v79, %[ws], %[opx] offen offset:1024 sc1\nbuffer_store_dwordx4 v[82:85], v79, %[ws], %[opx] offen offset:2048 sc1\nbuffer_store_dwordx4 v[82:85], v88, %[ws], %[opx] offen offset:3072 sc1\n"
            "v_mfma_f32_32x32x4_2b_bf16 v[96:127], v[60:61], v[74:75], 0\nds_read_b32 v60, v77 offset:37888\n"
            "v_exp_f32 v0, v0\nv_exp_f32 v1, v1\nv_exp_f32 v2, v2\nv_exp_f32 v3, v3\nv_cvt_pk_bf16_f32 v48, v0, v1\nv_cvt_pk_bf16_f32 v49, v2, v3\n"
            "v_exp_f32 v4, v4\nv_exp_f32 v5, v5\nv_exp_f32 v6, v6\nv_exp_f32 v7, v7\nv_cvt_pk_bf16_f32 v50, v4, v5\nv_cvt_pk_bf16_f32 v51, v6, v7\n"
            "s_waitcnt lgkmcnt(1)\nv_mfma_f32_32x32x16_bf16 v[32:47], v[66:69], v[52:55], v[32:47]\n"
            "v_exp_f32 v8, v8\nv_exp_f32 v9, v9\nv_exp_f32 v10, v10\nv_exp_f32 v11, v11\nv_cvt_pk_bf16_f32 v52, v8, v9\nv_cvt_pk_bf16_f32 v53, v10, v11\n"
            "v_mfma_f32_32x32x16_bf16 v[32:47], v[62:65], v[48:51], v[32:47]\ns_mov_b64 exec, s[36:37]\nds_read_b128 v[66:69], v78 offset:24416\nds_read_b128 v[62:65], v78 offset:25024\ns_mov_b64 exec, -1\n"
            "v_exp_f32 v12, v12\nv_exp_f32 v13, v13\nv_exp_f32 v14, v14\nv_exp_f32 v15, v15\nv_cvt_pk_bf16_f32 v54, v12, v13\nv_cvt_pk_bf16_f32 v55, v14, v15\n"
            "v_mfma_f32_32x32x16_bf16 v[96:111], v[56:59], v[70:73], v[96:111]\nds_read2st64_b64 v[56:59], v76 offset0:74 offset1:75\n"
            "v_exp_f32 v16, v16\nv_exp_f32 v17, v17\nv_exp_f32 v18, v18\nv_exp_f32 v19, v19\nv_cvt_pk_bf16_f32 v48, v16, v17\nv_cvt_pk_bf16_f32 v49, v18, v19\n"
            "v_mfma_f32_32x32x16_bf16 v[112:127], v[90:93], v[70:73], v[112:127]\nds_read2st64_b64 v[90:93], v76 offset0:76 offset1:77\n"
            "v_exp_f32 v20, v20\nv_exp_f32 v21, v21\nv_exp_f32 v22, v22\nv_exp_f32 v23, v23\nv_cvt_pk_bf16_f32 v50, v20, v21\nv_cvt_pk_bf16_f32 v51, v22, v23\n"
            "s_waitcnt lgkmcnt(2)\nv_mfma_f32_32x32x16_bf16 v[32:47], v[66:69], v[52:55], v[32:47]\n"
            "v_exp_f32 v24, v24\nv_exp_f32 v25, v25\nv_exp_f32 v26, v26\nv_exp_f32 v27, v27\nv_cvt_pk_bf16_f32 v52, v24, v25\nv_cvt_pk_bf16_f32 v53, v26, v27\n"
            "v_mfma_f32_32x32x16_bf16 v[32:47], v[62:65], v[48:51], v[32:47]\ns_mov_b64 exec, s[36:37]\nds_read_b128 v[66:69], v78 offset:25632\nds_read_b128 v[62:65], v78 offset:26240\ns_mov_b64 exec, -1\n"
            "v_exp_f32 v28, v28\nv_exp_f32 v29, v29\nv_exp_f32 v30, v30\nv_exp_f32 v31, v31\nv_cvt_pk_bf16_f32 v54, v28, v29\nv_cvt_pk_bf16_f32 v55, v30, v31\n"
            "v_mfma_f32_32x32x4_2b_bf16 v[0:31], v[60:61], v[74:75], 0\nds_read_b32 v60, v77 offset:38144\n"
            "v_exp_f32 v96, v96\nv_exp_f32 v97, v97\nv_exp_f32 v98, v98\nv_exp_f32 v99, v99\nv_cvt_pk_bf16_f32 v48, v96, v97\nv_cvt_pk_bf16_f32 v49, v98, v99\n"
            "v_exp_f32 v100, v100\nv_exp_f32 v101, v101\nv_exp_f32 v102, v102\nv_exp_f32 v103, v103\nv_cvt_pk_bf16_f32 v50, v100, v101\nv_cvt_pk_bf16_f32 v51, v102, v103\n"
            "s_waitcnt lgkmcnt(1)\nv_mfma_f32_32x32x16_bf16 v[32:47], v[66:69], v[52:55], v[32:47]\n"
            "v_exp_f32 v104, v104\nv_exp_f32 v105, v105\nv_exp_f32 v106, v106\nv_exp_f32 v107, v107\nv_cvt_pk_bf16_f32 v52, v104, v105\nv_cvt_pk_bf16_f32 v53, v106, v107\n"
            "v_mfma_f32_32x32x16_bf16 v[32:47], v[62:65], v[48:51], v[32:47]\ns_mov_b64 exec, s[36:37]\nds_read_b128 v[66:69], v78 offset:26848\nds_read_b128 v[62:65], v78 offset:27456\ns_mov_b64 exec, -1\n"
            "v_exp_f32 v108, v108\nv_exp_f32 v109, v109\nv_exp_f32 v110, v110\nv_exp_f32 v111, v111\nv_cvt_pk_bf16_f32 v54, v108, v109\nv_cvt_pk_bf16_f32 v55, v110, v111\n"
            "v_mfma_f32_32x32x16_bf16 v[0:15], v[56:59], v[70:73], v[0:15]\nds_read2st64_b64 v[56:59], v76 offset0:78 offset1:79\n"
            "v_exp_f32 v112, v112\nv_exp_f32 v113, v113\nv_exp_f32 v114, v114\nv_exp_f32 v115, v115\nv_cvt_pk_bf16_f32 v48, v112, v113\nv_cvt_pk_bf16_f32 v49, v114, v115\n"
            "v_mfma_f32_32x32x16_bf16 v[16:31], v[90:93], v[70:73], v[16:31]\nds_read2st64_b64 v[90:93], v76 offset0:80 offset1:81\n"
            "v_exp_f32 v116, v116\nv_exp_f32 v117, v117\nv_exp_f32 v118, v118\nv_exp_f32 v119, v119\nv_cvt_pk_bf16_f32 v50, v116, v117\nv_cvt_pk_bf16_f32 v51, v118, v119\n"
            "s_waitcnt lgkmcnt(2)\nv_mfma_f32_32x32x16_bf16 v[32:47], v[66:69], v[52:55], v[32:47]\n"
            "v_exp_f32 v120, v120\nv_exp_f32 v121, v121\nv_exp_f32 v122, v122\nv_exp_f32 v123, v123\nv_cvt_pk_bf16_f32 v52, v120, v121\nv_cvt_pk_bf16_f32 v53, v122, v123\n"
            "v_mfma_f32_32x32x16_bf16 v[32:47], v[62:65], v[48:51], v[32:47]\ns_mov_b64 exec, s[36:37]\nds_read_b128 v[66:69], v78 offset:28064\nds_read_b128 v[62:65], v78 offset:37888\ns_mov_b64 exec, -1\n"
            "v_exp_f32 v124, v124\nv_exp_f32 v125, v125\nv_exp_f32 v126, v126\nv_exp_f32 v127, v127\nv_cvt_pk_bf16_f32 v54, v124, v125\nv_cvt_pk_bf16_f32 v55, v126, v127\n"
            "v_add_u32 v76, 0x9400, v76\nv_add_u32 v77, 0x9400, v77\nv_add_u32 v78, 0x9400, v78\n.p2align 6\n2:\ns_cmp_lg_u32 s38, 6\ns_cbranch_scc1 6f\ns_cbranch_vccz 7f\ns_waitcnt lgkmcnt(0)\ns_barrier\ns_branch 6f\n7:\nbuffer_load_dwordx2 v[82:83], v89, %[ws], %[fw] offen sc1\n6:\ns_cmp_ge_u32 s38, %[pfn]\ns_cbranch_scc1 3f\nbuffer_load_dword v81, v80, %[pf], s41 offen sc1\ns_add_u32 s41, s41, %[pfs]\n3:\n"
            "v_mfma_f32_32x32x4_2b_bf16 v[96:127], v[60:61], v[74:75], 0\nds_read_b32 v60, v77 offset:512\n"
            "v_exp_f32 v0, v0\nv_exp_f32 v1, v1\nv_exp_f32 v2, v2\nv_exp_f32 v3, v3\nv_cvt_pk_bf16_f32 v48, v0, v1\nv_cvt_pk_bf16_f32 v49, v2, v3\n"
            "v_exp_f32 v4, v4\nv_exp_f32 v5, v5\nv_exp_f32 v6, v6\nv_exp_f32 v7, v7\nv_cvt_pk_bf16_f32 v50, v4, v5\nv_cvt_pk_bf16_f32 v51, v6, v7\n"
            "s_waitcnt lgkmcnt(1)\nv_mfma_f32_32x32x16_bf16 v[32:47], v[66:69], v[52:55], v[32:47]\n"
            "v_exp_f32 v8, v8\nv_exp_f32 v9, v9\nv_exp_f32 v10, v10\nv_exp_f32 v11, v11\nv_cvt_pk_bf16_f32 v52, v8, v9\nv_cvt_pk_bf16_f32 v53, v10, v11\n"
            "v_mfma_f32_32x32x16_bf16 v[32:47], v[62:65], v[48:51], v[32:47]\ns_mov_b64 exec, s[36:37]\nds_read_b128 v[66:69], v78 offset:608\nds_read_b128 v[62:65], v78 offset:1216\ns_mov_b64 exec, -1\n"
            "v_exp_f32 v12, v12\nv_exp_f32 v13, v13\nv_exp_f32 v14, v14\nv_exp_f32 v15, v15\nv_cvt_pk_bf16_f32 v54, v12, v13\nv_cvt_pk_bf16_f32 v55, v14, v15\n"
            "v_mfma_f32_32x32x16_bf16 v[96:111], v[56:59], v[70:73], v[96:111]\nds_read2st64_b64 v[56:59], v76 offset0:8 offset1:9\n"
            "v_exp_f32 v16, v16\nv_exp_f32 v17, v17\nv_exp_f32 v18, v18\nv_exp_f32 v19, v19\nv_cvt_pk_bf16_f32 v48, v16, v17\nv_cvt_pk_bf16_f32 v49, v18, v19\n"
            "v_mfma_f32_32x32x16_bf16 v[112:127], v[90:93], v[70:73], v[112:127]\nds_read2st64_b64 v[90:93], v76 offset0:10 offset1:11\n"
            "v_exp_f32 v20, v20\nv_exp_f32 v21, v21\nv_exp_f32 v22, v22\nv_exp_f32 v23, v23\nv_cvt_pk_bf16_f32 v50, v20, v21\nv_cvt_pk_bf16_f32 v51, v22, v23\n"
            "s_waitcnt lgkmcnt(2)\nv_mfma_f32_32x32x16_bf16 v[32:47], v[66:69], v[52:55], v[32:47]\n"
            "v_exp_f32 v24, v24\nv_exp_f32 v25, v25\nv_exp_f32 v26, v26\nv_exp_f32 v27, v27\nv_cvt_pk_bf16_f32 v52, v24, v25\nv_cvt_pk_bf16_f32 v53, v26, v27\n"
            "v_mfma_f32_32x32x16_bf16 v[32:47], v[62:65], v[48:51], v[32:47]\ns_mov_b64 exec, s[36:37]\nds_read_b128 v[66:69], v78 offset:1824\nds_read_b128 v[62:65], v78 offset:2432\ns_mov_b64 exec, -1\n"
            "v_exp_f32 v28, v28\nv_exp_f32 v29, v29\nv_exp_f32 v30, v30\nv_exp_f32 v31, v31\nv_cvt_pk_bf16_f32 v54, v28, v29\nv_cvt_pk_bf16_f32 v55, v30, v31\n"
            "v_mfma_f32_32x32x4_2b_bf16 v[0:31], v[60:61], v[74:75], 0\nds_read_b32 v60, v77 offset:768\n"
            "v_exp_f32 v96, v96\nv_exp_f32 v97, v97\nv_exp_f32 v98, v98\nv_exp_f32 v99, v99\nv_cvt_pk_bf16_f32 v48, v96, v97\nv_cvt_pk_bf16_f32 v49, v98, v99\n"
            "v_exp_f32 v100, v100\nv_exp_f32 v101, v101\nv_exp_f32 v102, v102\nv_exp_f32 v103, v103\nv_cvt_pk_bf16_f32 v50, v100, v101\nv_cvt_pk_bf16_f32 v51, v102, v103\n"
            "s_waitcnt lgkmcnt(1)\nv_mfma_f32_32x32x16_bf16 v[32:47], v[66:69], v[52:55], v[32:47]\n"
            "v_exp_f32 v104, v104\nv_exp_f32 v105, v105\nv_exp_f32 v106, v106\nv_exp_f32 v107, v107\nv_cvt_pk_bf16_f32 v52, v104, v105\nv_cvt_pk_bf16_f32 v53, v106, v107\n"
            "v_mfma_f32_32x32x16_bf16 v[32:47], v[62:65], v[48:51], v[32:47]\ns_mov_b64 exec, s[36:37]\nds_read_b128 v[66:69], v78 offset:3040\nds_read_b128 v[62:65], v78 offset:3648\ns_mov_b64 exec, -1\n"
            "v_exp_f32 v108, v108\nv_exp_f32 v109, v109\nv_exp_f32 v110, v110\nv_exp_f32 v111, v111\nv_cvt_pk_bf16_f32 v54, v108, v109\nv_cvt_pk_bf16_f32 v55, v110, v111\n"
            "v_mfma_f32_32x32x16_bf16 v[0:15], v[56:59], v[70:73], v[0:15]\nds_read2st64_b64 v[56:59], v76 offset0:12 offset1:13\n"
            "v_exp_f32 v112, v112\nv_exp_f32 v113, v113\nv_exp_f32 v114, v114\nv_exp_f32 v115, v115\nv_cvt_pk_bf16_f32 v48, v112, v113\nv_cvt_pk_bf16_f32 v49, v114, v115\n"
            "v_mfma_f32_32x32x16_bf16 v[16:31], v[90:93], v[70:73], v[16:31]\nds_read2st64_b64 v[90:93], v76 offset0:14 offset1:15\n"
            "v_exp_f32 v116, v116\nv_exp_f32 v117, v117\nv_exp_f32 v118, v118\nv_exp_f32 v119, v119\nv_cvt_pk_bf16_f32 v50, v116, v117\nv_cvt_pk_bf16_f32 v51, v118, v119\n"
            "s_waitcnt lgkmcnt(2)\nv_mfma_f32_32x32x16_bf16 v[32:47], v[66:69], v[52:55], v[32:47]\n"
            "v_exp_f32 v120, v120\nv_exp_f32 v121, v121\nv_exp_f32 v122, v122\nv_exp_f32 v123, v123\nv_cvt_pk_bf16_f32 v52, v120, v121\nv_cvt_pk_bf16_f32 v53, v122, v123\n"
            "v_mfma_f32_32x32x16_bf16 v[32:47], v[62:65], v[48:51], v[32:47]\ns_mov_b64 exec, s[36:37]\nds_read_b128 v[66:69], v78 offset:4256\nds_read_b128 v[62:65], v78 offset:4864\ns_mov_b64 exec, -1\n"
            "v_exp_f32 v124, v124\nv_exp_f32 v125, v125\nv_exp_f32 v126, v126\nv_exp_f32 v127, v127\nv_cvt_pk_bf16_f32 v54, v124, v125\nv_cvt_pk_bf16_f32 v55, v126, v127\n"
            "v_mfma_f32_32x32x4_2b_bf16 v[96:127], v[60:61], v[74:75], 0\nds_read_b32 v60, v77 offset:18944\n"
            "v_exp_f32 v0, v0\nv_exp_f32 v1, v1\nv_exp_f32 v2, v2\nv_exp_f32 v3, v3\nv_cvt_pk_bf16_f32 v48, v0, v1\nv_cvt_pk_bf16_f32 v49, v2, v3\n"
            "v_exp_f32 v4, v4\nv_exp_f32 v5, v5\nv_exp_f32 v6, v6\nv_exp_f32 v7, v7\nv_cvt_pk_bf16_f32 v50, v4, v5\nv_cvt_pk_bf16_f32 v51, v6, v7\n"
            "s_waitcnt lgkmcnt(1)\nv_mfma_f32_32x32x16_bf16 v[32:47], v[66:69], v[52:55], v[32:47]\n"
            "v_exp_f32 v8, v8\nv_exp_f32 v9, v9\nv_exp_f32 v10, v10\nv_exp_f32 v11, v11\nv_cvt_pk_bf16_f32 v52, v8, v9\nv_cvt_pk_bf16_f32 v53, v10, v11\n"
            "v_mfma_f32_32x32x16_bf16 v[32:47], v[62:65], v[48:51], v[32:47]\ns_mov_b64 exec, s[36:37]\nds_read_b128 v[66:69], v78 offset:5472\nds_read_b128 v[62:65], v78 offset:6080\ns_mov_b64 exec, -1\n"
            "v_exp_f32 v12, v12\nv_exp_f32 v13, v13\nv_exp_f32 v14, v14\nv_exp_f32 v15, v15\nv_cvt_pk_bf16_f32 v54, v12, v13\nv_cvt_pk_bf16_f32 v55, v14, v15\n"
            "s_cmp_lg_u32 s38, 6\ns_cbranch_scc1 4f\ns_cbranch_vccnz 26f\ns_setprio 0\n26:\n4:\n"
            "v_mfma_f32_32x32x16_bf16 v[96:111], v[56:59], v[70:73], v[96:111]\nds_read2st64_b64 v[56:59], v76 offset0:37 offset1:38\n"
            "v_exp_f32 v16, v16\nv_exp_f32 v17, v17\nv_exp_f32 v18, v18\nv_exp_f32 v19, v19\nv_cvt_pk_bf16_f32 v48, v16, v17\nv_cvt_pk_bf16_f32 v49, v18, v19\n"
            "v_mfma_f32_32x32x16_bf16 v[112:127], v[90:93], v[70:73], v[112:127]\nds_read2st64_b64 v[90:93], v76 offset0:39 offset1:40\n"
            "v_exp_f32 v20, v20\nv_exp_f32 v21, v21\nv_exp_f32 v22, v22\nv_exp_f32 v23, v23\nv_cvt_pk_bf16_f32 v50, v20, v21\nv_cvt_pk_bf16_f32 v51, v22, v23\n"
            "s_waitcnt lgkmcnt(2)\nv_mfma_f32_32x32x16_bf16 v[32:47], v[66:69], v[52:55], v[32:47]\n"
            "v_exp_f32 v24, v24\nv_exp_f32 v25, v25\nv_exp_f32 v26, v26\nv_exp_f32 v27, v27\nv_cvt_pk_bf16_f32 v52, v24, v25\nv_cvt_pk_bf16_f32 v53, v26, v27\n"
            "v_mfma_f32_32x32x16_bf16 v[32:47], v[62:65], v[48:51], v[32:47]\ns_mov_b64 exec, s[36:37]\nds_read_b128 v[66:69], v78 offset:6688\nds_read_b128 v[62:65], v78 offset:7296\ns_mov_b64 exec, -1\n"
            "v_exp_f32 v28, v28\nv_exp_f32 v29, v29\nv_exp_f32 v30, v30\nv_exp_f32 v31, v31\nv_cvt_pk_bf16_f32 v54, v28, v29\nv_cvt_pk_bf16_f32 v55, v30, v31\n"
            "v_mfma_f32_32x32x4_2b_bf16 v[0:31], v[60:61], v[74:75], 0\nds_read_b32 v60, v77 offset:19200\n"
            "v_exp_f32 v96, v96\nv_exp_f32 v97, v97\nv_exp_f32 v98, v98\nv_exp_f32 v99, v99\nv_cvt_pk_bf16_f32 v48, v96, v97\nv_cvt_pk_bf16_f32 v49, v98, v99\n"
            "v_exp_f32 v100, v100\nv_exp_f32 v101, v101\nv_exp_f32 v102, v102\nv_exp_f32 v103, v103\nv_cvt_pk_bf16_f32 v50, v100, v101\nv_cvt_pk_bf16_f32 v51, v102, v103\n"
            "s_waitcnt lgkmcnt(1)\nv_mfma_f32_32x32x16_bf16 v[32:47], v[66:69], v[52:55], v[32:47]\n"
            "v_exp_f32 v104, v104\nv_exp_f32 v105, v105\nv_exp_f32 v106, v106\nv_exp_f32 v107, v107\nv_cvt_pk_bf16_f32 v52, v104, v105\nv_cvt_pk_bf16_f32 v53, v106, v107\n"
            "v_mfma_f32_32x32x16_bf16 v[32:47], v[62:65], v[48:51], v[32:47]\ns_mov_b64 exec, s[36:37]\nds_read_b128 v[66:69], v78 offset:7904\nds_read_b128 v[62:65], v78 offset:8512\ns_mov_b64 exec, -1\n"
            "v_exp_f32 v108, v108\nv_exp_f32 v109, v109\nv_exp_f32 v110, v110\nv_exp_f32 v111, v111\nv_cvt_pk_bf16_f32 v54, v108, v109\nv_cvt_pk_bf16_f32 v55, v110, v111\n"
            "v_mfma_f32_32x32x16_bf16 v[0:15], v[56:59], v[70:73], v[0:15]\nds_read2st64_b64 v[56:59], v76 offset0:41 offset1:42\n"
            "v_exp_f32 v112, v112\nv_exp_f32 v113, v113\nv_exp_f32 v114, v114\nv_exp_f32 v115, v115\nv_cvt_pk_bf16_f32 v48, v112, v113\nv_cvt_pk_bf16_f32 v49, v114, v115\n"
            "v_mfma_f32_32x32x16_bf16 v[16:31], v[90:93], v[70:73], v[16:31]\nds_read2st64_b64 v[90:93], v76 offset0:43 offset1:44\n"
            "v_exp_f32 v116, v116\nv_exp_f32 v117, v117\nv_exp_f32 v118, v118\nv_exp_f32 v119, v119\nv_cvt_pk_bf16_f32 v50, v116, v117\nv_cvt_pk_bf16_f32 v51, v118, v119\n"
            "s_waitcnt lgkmcnt(2)\nv_mfma_f32_32x32x16_bf16 v[32:47], v[66:69], v[52:55], v[32:47]\n"
            "v_exp_f32 v120, v120\nv_exp_f32 v121, v121\nv_exp_f32 v122, v122\nv_exp_f32 v123, v123\nv_cvt_pk_bf16_f32 v52, v120, v121\nv_cvt_pk_bf16_f32 v53, v122, v123\n"
            "v_mfma_f32_32x32x16_bf16 v[32:47], v[62:65], v[48:51], v[32:47]\ns_mov_b64 exec, s[36:37]\nds_read_b128 v[66:69], v78 offset:9120\nds_read_b128 v[62:65], v78 offset:18944\ns_mov_b64 exec, -1\n"
            "v_exp_f32 v124, v124\nv_exp_f32 v125, v125\nv_exp_f32 v126, v126\nv_exp_f32 v127, v127\nv_cvt_pk_bf16_f32 v54, v124, v125\nv_cvt_pk_bf16_f32 v55, v126, v127\n"
            "v_add_u32 v76, 0x4a00, v76\nv_add_u32 v77, 0x4a00, v77\nv_add_u32 v78, 0x4a00, v78\ns_add_u32 s38, s38, 1\ns_cmp_lt_u32 s38, 7\ns_cbranch_scc1 2b\n"
            "s_cbranch_vccnz 10f\n"
            "v_mfma_f32_32x32x4_2b_bf16 v[96:127], v[60:61], v[74:75], 0\nds_read_b32 v60, v77 offset:512\n"
            "v_exp_f32 v0, v0\nv_exp_f32 v1, v1\nv_exp_f32 v2, v2\nv_exp_f32 v3, v3\nv_cvt_pk_bf16_f32 v48, v0, v1\nv_cvt_pk_bf16_f32 v49, v2, v3\n"
            "v_exp_f32 v4, v4\nv_exp_f32 v5, v5\nv_exp_f32 v6, v6\nv_exp_f32 v7, v7\nv_cvt_pk_bf16_f32 v50, v4, v5\nv_cvt_pk_bf16_f32 v51, v6, v7\n"
            "s_waitcnt lgkmcnt(1)\nv_mfma_f32_32x32x16_bf16 v[32:47], v[66:69], v[52:55], v[32:47]\n"
            "v_exp_f32 v8, v8\nv_exp_f32 v9, v9\nv_exp_f32 v10, v10\nv_exp_f32 v11, v11\nv_cvt_pk_bf16_f32 v52, v8, v9\nv_cvt_pk_bf16_f32 v53, v10, v11\n"
            "v_mfma_f32_32x32x16_bf16 v[32:47], v[62:65], v[48:51], v[32:47]\ns_mov_b64 exec, s[36:37]\nds_read_b128 v[66:69], v78 offset:608\nds_read_b128 v[62:65], v78 offset:1216\ns_mov_b64 exec, -1\n"
            "v_exp_f32 v12, v12\nv_exp_f32 v13, v13\nv_exp_f32 v14, v14\nv_exp_f32 v15, v15\nv_cvt_pk_bf16_f32 v54, v12, v13\nv_cvt_pk_bf16_f32 v55, v14, v15\n"
            "v_mfma_f32_32x32x16_bf16 v[96:111], v[56:59], v[70:73], v[96:111]\nds_read2st64_b64 v[56:59], v76 offset0:8 offset1:9\n"
            "v_exp_f32 v16, v16\nv_exp_f32 v17, v17\nv_exp_f32 v18, v18\nv_exp_f32 v19, v19\nv_cvt_pk_bf16_f32 v48, v16, v17\nv_cvt_pk_bf16_f32 v49, v18, v19\n"
            "v_mfma_f32_32x32x16_bf16 v[112:127], v[90:93], v[70:73], v[112:127]\nds_read2st64_b64 v[90:93], v76 offset0:10 offset1:11\n"
            "v_exp_f32 v20, v20\nv_exp_f32 v21, v21\nv_exp_f32 v22, v22\nv_exp_f32 v23, v23\nv_cvt_pk_bf16_f32 v50, v20, v21\nv_cvt_pk_bf16_f32 v51, v22, v23\n"
            "s_waitcnt lgkmcnt(2)\nv_mfma_f32_32x32x16_bf16 v[32:47], v[66:69], v[52:55], v[32:47]\n"
            "v_exp_f32 v24, v24\nv_exp_f32 v25, v25\nv_exp_f32 v26, v26\nv_exp_f32 v27, v27\nv_cvt_pk_bf16_f32 v52, v24, v25\nv_cvt_pk_bf16_f32 v53, v26, v27\n"
            "v_mfma_f32_32x32x16_bf16 v[32:47], v[62:65], v[48:51], v[32:47]\ns_mov_b64 exec, s[36:37]\nds_read_b128 v[66:69], v78 offset:1824\nds_read_b128 v[62:65], v78 offset:2432\ns_mov_b64 exec, -1\n"
            "v_exp_f32 v28, v28\nv_exp_f32 v29, v29\nv_exp_f32 v30, v30\nv_exp_f32 v31, v31\nv_cvt_pk_bf16_f32 v54, v28, v29\nv_cvt_pk_bf16_f32 v55, v30, v31\n"
            "v_mfma_f32_32x32x4_2b_bf16 v[0:31], v[60:61], v[74:75], 0\nds_read_b32 v60, v77 offset:768\n"
            "v_exp_f32 v96, v96\nv_exp_f32 v97, v97\nv_exp_f32 v98, v98\nv_exp_f32 v99, v99\nv_cvt_pk_bf16_f32 v48, v96, v97\nv_cvt_pk_bf16_f32 v49, v98, v99\n"
            "v_exp_f32 v100, v100\nv_exp_f32 v101, v101\nv_exp_f32 v102, v102\nv_exp_f32 v103, v103\nv_cvt_pk_bf16_f32 v50, v100, v101\nv_cvt_pk_bf16_f32 v51, v102, v103\n"
            "s_waitcnt lgkmcnt(1)\nv_mfma_f32_32x32x16_bf16 v[32:47], v[66:69], v[52:55], v[32:47]\n"
            "v_exp_f32 v104, v104\nv_exp_f32 v105, v105\nv_exp_f32 v106, v106\nv_exp_f32 v107, v107\nv_cvt_pk_bf16_f32 v52, v104, v105\nv_cvt_pk_bf16_f32 v53, v106, v107\n"
            "v_mfma_f32_32x32x16_bf16 v[32:47], v[62:65], v[48:51], v[32:47]\ns_mov_b64 exec, s[36:37]\nds_read_b128 v[66:69], v78 offset:3040\nds_read_b128 v[62:65], v78 offset:3648\ns_mov_b64 exec, -1\n"
            "v_exp_f32 v108, v108\nv_exp_f32 v109, v109\nv_exp_f32 v110, v110\nv_exp_f32 v111, v111\nv_cvt_pk_bf16_f32 v54, v108, v109\nv_cvt_pk_bf16_f32 v55, v110, v111\n"
            "v_mfma_f32_32x32x16_bf16 v[0:15], v[56:59], v[70:73], v[0:15]\nds_read2st64_b64 v[56:59], v76 offset0:12 offset1:13\n"
            "v_exp_f32 v112, v112\nv_exp_f32 v113, v113\nv_exp_f32 v114, v114\nv_exp_f32 v115, v115\nv_cvt_pk_bf16_f32 v48, v112, v113\nv_cvt_pk_bf16_f32 v49, v114, v115\n"
            "v_mfma_f32_32x32x16_bf16 v[16:31], v[90:93], v[70:73], v[16:31]\nds_read2st64_b64 v[90:93], v76 offset0:14 offset1:15\n"
            "v_exp_f32 v116, v116\nv_exp_f32 v117, v117\nv_exp_f32 v118, v118\nv_exp_f32 v119, v119\nv_cvt_pk_bf16_f32 v50, v116, v117\nv_cvt_pk_bf16_f32 v51, v118, v119\n"
            "s_waitcnt lgkmcnt(2)\nv_mfma_f32_32x32x16_bf16 v[32:47], v[66:69], v[52:55], v[32:47]\n"
            "v_exp_f32 v120, v120\nv_exp_f32 v121, v121\nv_exp_f32 v122, v122\nv_exp_f32 v123, v123\nv_cvt_pk_bf16_f32 v52, v120, v121\nv_cvt_pk_bf16_f32 v53, v122, v123\n"
            "v_mfma_f32_32x32x16_bf16 v[32:47], v[62:65], v[48:51], v[32:47]\ns_mov_b64 exec, s[36:37]\nds_read_b128 v[66:69], v78 offset:4256\nds_read_b128 v[62:65], v78 offset:4864\ns_mov_b64 exec, -1\n"
            "v_exp_f32 v124, v124\nv_exp_f32 v125, v125\nv_exp_f32 v126, v126\nv_exp_f32 v127, v127\nv_cvt_pk_bf16_f32 v54, v124, v125\nv_cvt_pk_bf16_f32 v55, v126, v127\n"
            "v_mfma_f32_32x32x4_2b_bf16 v[96:127], v[60:61], v[74:75], 0\nds_read_b32 v60, v77 offset:18944\n"
            "v_exp_f32 v0, v0\nv_exp_f32 v1, v1\nv_exp_f32 v2, v2\nv_exp_f32 v3, v3\nv_cvt_pk_bf16_f32 v48, v0, v1\nv_cvt_pk_bf16_f32 v49, v2, v3\n"
            "v_exp_f32 v4, v4\nv_exp_f32 v5, v5\nv_exp_f32 v6, v6\nv_exp_f32 v7, v7\nv_cvt_pk_bf16_f32 v50, v4, v5\nv_cvt_pk_bf16_f32 v51, v6, v7\n"
            "s_waitcnt lgkmcnt(1)\nv_mfma_f32_32x32x16_bf16 v[32:47], v[66:69], v[52:55], v[32:47]\n"
            "v_exp_f32 v8, v8\nv_exp_f32 v9, v9\nv_exp_f32 v10, v10\nv_exp_f32 v11, v11\nv_cvt_pk_bf16_f32 v52, v8, v9\nv_cvt_pk_bf16_f32 v53, v10, v11\n"
            "v_mfma_f32_32x32x16_bf16 v[32:47], v[62:65], v[48:51], v[32:47]\ns_mov_b64 exec, s[36:37]\nds_read_b128 v[66:69], v78 offset:5472\nds_read_b128 v[62:65], v78 offset:6080\ns_mov_b64 exec, -1\n"
            "v_exp_f32 v12, v12\nv_exp_f32 v13, v13\nv_exp_f32 v14, v14\nv_exp_f32 v15, v15\nv_cvt_pk_bf16_f32 v54, v12, v13\nv_cvt_pk_bf16_f32 v55, v14, v15\n"
            "s_barrier\ns_waitcnt vmcnt(0)\nv_cmp_eq_u32 s[42:43], %[tl], v82\nv_cmp_eq_u32 s[44:45], %[th], v83\ns_and_b64 s[42:43], s[42:43], s[44:45]\ns_cmp_eq_u64 s[42:43], -1\ns_cbranch_scc0 11f\ns_mov_b32 %[early], 1\ns_mov_b32 s39, %[wp]\ns_sub_u32 s40, %[p0], 0x6000\ns_add_u32 s40, s40, %[lds]\n"
            "v_mfma_f32_32x32x16_bf16 v[96:111], v[56:59], v[70:73], v[96:111]\nds_read2st64_b64 v[56:59], v76 offset0:37 offset1:38\n"
            "v_exp_f32 v16, v16\nv_exp_f32 v17, v17\nv_exp_f32 v18, v18\nv_exp_f32 v19, v19\nv_cvt_pk_bf16_f32 v48, v16, v17\nv_cvt_pk_bf16_f32 v49, v18, v19\n"
            "v_mfma_f32_32x32x16_bf16 v[112:127], v[90:93], v[70:73], v[112:127]\nds_read2st64_b64 v[90:93], v76 offset0:39 offset1:40\ns_mov_b32 m0, s40\ns_add_u32 s40, s40, 0x1000\nbuffer_load_dwordx4 v79, %[ws], s39 offen sc1 lds\ns_add_u32 s39, s39, 0x1000\ns_mov_b32 m0, s40\ns_add_u32 s40, s40, 0x1000\nbuffer_load_dwordx4 v79, %[ws], s39 offen sc1 lds\ns_add_u32 s39, s39, 0x1000\ns_mov_b32 m0, s40\ns_add_u32 s40, s40, 0x1000\nbuffer_load_dwordx4 v79, %[ws], s39 offen sc1 lds\ns_add_u32 s39, s39, 0x1000\ns_mov_b32 m0, s40\ns_add_u32 s40, s40, 0x1000\nbuffer_load_dwordx4 v79, %[ws], s39 offen sc1 lds\ns_add_u32 s39, s39, 0x1000\ns_mov_b32 m0, s40\ns_add_u32 s40, s40, 0x1000\nbuffer_load_dwordx4 v79, %[ws], s39 offen sc1 lds\ns_add_u32 s39, s39, 0x1000\ns_mov_b32 m0, s40\ns_add_u32 s40, s40, 0x1000\nbuffer_load_dwordx4 v79, %[ws], s39 offen sc1 lds\ns_add_u32 s39, s39, 0x1000\ns_mov_b32 m0, s40\ns_add_u32 s40, s40, 0x1000\nbuffer_load_dwordx4 v79, %[ws], s39 offen sc1 lds\ns_add_u32 s39, s39, 0x1000\n"
            "v_exp_f32 v20, v20\nv_exp_f32 v21, v21\nv_exp_f32 v22, v22\nv_exp_f32 v23, v23\nv_cvt_pk_bf16_f32 v50, v20, v21\nv_cvt_pk_bf16_f32 v51, v22, v23\n"
            "s_waitcnt lgkmcnt(2)\nv_mfma_f32_32x32x16_bf16 v[32:47], v[66:69], v[52:55], v[32:47]\ns_mov_b32 m0, s40\ns_add_u32 s40, s40, 0x1000\nbuffer_load_dwordx4 v79, %[ws], s39 offen sc1 lds\ns_add_u32 s39, s39, 0x1000\ns_mov_b32 m0, s40\ns_add_u32 s40, s40, 0x1000\nbuffer_load_dwordx4 v79, %[ws], s39 offen sc1 lds\ns_add_u32 s39, s39, 0x1000\ns_mov_b32 m0, s40\ns_add_u32 s40, s40, 0x1000\nbuffer_load_dwordx4 v79, %[ws], s39 offen sc1 lds\ns_add_u32 s39, s39, 0x1000\ns_mov_b32 m0, s40\ns_add_u32 s40, s40, 0x1000\nbuffer_load_dwordx4 v79, %[ws], s39 offen sc1 lds\ns_add_u32 s39, s39, 0x1000\ns_mov_b32 m0, s40\ns_add_u32 s40, s40, 0x1000\nbuffer_load_dwordx4 v79, %[ws], s39 offen sc1 lds\ns_add_u32 s39, s39, 0x1000\ns_mov_b32 m0, s40\ns_add_u32 s40, s40, 0x1000\nbuffer_load_dwordx4 v79, %[ws], s39 offen sc1 lds\ns_add_u32 s39, s39, 0x1000\ns_mov_b32 m0, s40\ns_add_u32 s40, s40, 0x1000\nbuffer_load_dwordx4 v79, %[ws], s39 offen sc1 lds\ns_add_u32 s39, s39, 0x1000\n"
            "v_exp_f32 v24, v24\nv_exp_f32 v25, v25\nv_exp_f32 v26, v26\nv_exp_f32 v27, v27\nv_cvt_pk_bf16_f32 v52, v24, v25\nv_cvt_pk_bf16_f32 v53, v26, v27\n"
            "v_mfma_f32_32x32x16_bf16 v[32:47], v[62:65], v[48:51], v[32:47]\ns_mov_b64 exec, s[36:37]\nds_read_b128 v[66:69], v78 offset:6688\nds_read_b128 v[62:65], v78 offset:7296\ns_mov_b64 exec, -1\n"
            "v_exp_f32 v28, v28\nv_exp_f32 v29, v29\nv_exp_f32 v30, v30\nv_exp_f32 v31, v31\nv_cvt_pk_bf16_f32 v54, v28, v29\nv_cvt_pk_bf16_f32 v55, v30, v31\n"
            "v_mfma_f32_32x32x4_2b_bf16 v[0:31], v[60:61], v[74:75], 0\nds_read_b32 v60, v77 offset:19200\n"
            "v_exp_f32 v96, v96\nv_exp_f32 v97, v97\nv_exp_f32 v98, v98\nv_exp_f32 v99, v99\nv_cvt_pk_bf16_f32 v48, v96, v97\nv_cvt_pk_bf16_f32 v49, v98, v99\ns_mov_b32 m0, s40\ns_add_u32 s40, s40, 0x1000\nbuffer_load_dwordx4 v79, %[ws], s39 offen sc1 lds\ns_add_u32 s39, s39, 0x1000\ns_mov_b32 m0, s40\ns_add_u32 s40, s40, 0x1000\nbuffer_load_dwordx4 v79, %[ws], s39 offen sc1 lds\ns_add_u32 s39, s39, 0x1000\ns_mov_b32 m0, s40\ns_add_u32 s40, s40, 0x1000\nbuffer_load_dwordx4 v79, %[ws], s39 offen sc1 lds\ns_add_u32 s39, s39, 0x1000\ns_mov_b32 m0, s40\ns_add_u32 s40, s40, 0x1000\nbuffer_load_dwordx4 v79, %[ws], s39 offen sc1 lds\ns_add_u32 s39, s39, 0x1000\ns_mov_b32 m0, s40\ns_add_u32 s40, s40, 0x1000\nbuffer_load_dwordx4 v79, %[ws], s39 offen sc1 lds\ns_add_u32 s39, s39, 0x1000\ns_mov_b32 m0, s40\ns_add_u32 s40, s40, 0x1000\nbuffer_load_dwordx4 v79, %[ws], s39 offen sc1 lds\ns_add_u32 s39, s39, 0x1000\ns_mov_b32 m0, s40\ns_add_u32 s40, s40, 0x1000\nbuffer_load_dwordx4 v79, %[ws], s39 offen sc1 lds\ns_add_u32 s39, s39, 0x1000\n"
            "v_exp_f32 v100, v100\nv_exp_f32 v101, v101\nv_exp_f32 v102, v102\nv_exp_f32 v103, v103\nv_cvt_pk_bf16_f32 v50, v100, v101\nv_cvt_pk_bf16_f32 v51, v102, v103\n"
            "s_waitcnt lgkmcnt(1)\nv_mfma_f32_32x32x16_bf16 v[32:47], v[66:69], v[52:55], v[32:47]\ns_mov_b32 m0, s40\ns_add_u32 s40, s40, 0x1000\nbuffer_load_dwordx4 v79, %[ws], s39 offen sc1 lds\ns_add_u32 s39, s39, 0x1000\ns_mov_b32 m0, s40\ns_add_u32 s40, s40, 0x1000\nbuffer_load_dwordx4 v79, %[ws], s39 offen sc1 lds\ns_add_u32 s39, s39, 0x1000\ns_mov_b32 m0, s40\ns_add_u32 s40, s40, 0x1000\nbuffer_load_dwordx4 v79, %[ws], s39 offen sc1 lds\ns_add_u32 s39, s39, 0x1000\ns_mov_b32 m0, s40\ns_add_u32 s40, s40, 0x1000\nbuffer_load_dwordx4 v79, %[ws], s39 offen sc1 lds\ns_add_u32 s39, s39, 0x1000\ns_mov_b32 m0, s40\ns_add_u32 s40, s40, 0x1000\nbuffer_load_dwordx4 v79, %[ws], s39 offen sc1 lds\ns_add_u32 s39, s39, 0x1000\ns_mov_b32 m0, s40\ns_add_u32 s40, s40, 0x1000\nbuffer_load_dwordx4 v79, %[ws], s39 offen sc1 lds\ns_add_u32 s39, s39, 0x1000\n"
            "v_exp_f32 v104, v104\nv_exp_f32 v105, v105\nv_exp_f32 v106, v106\nv_exp_f32 v107, v107\nv_cvt_pk_bf16_f32 v52, v104, v105\nv_cvt_pk_bf16_f32 v53, v106, v107\n"
            "v_mfma_f32_32x32x16_bf16 v[32:47], v[62:65], v[48:51], v[32:47]\ns_mov_b64 exec, s[36:37]\nds_read_b128 v[66:69], v78 offset:7904\nds_read_b128 v[62:65], v78 offset:8512\ns_mov_b64 exec, -1\n"
            "v_exp_f32 v108, v108\nv_exp_f32 v109, v109\nv_exp_f32 v110, v110\nv_exp_f32 v111, v111\nv_cvt_pk_bf16_f32 v54, v108, v109\nv_cvt_pk_bf16_f32 v55, v110, v111\n"
            "v_mfma_f32_32x32x16_bf16 v[0:15], v[56:59], v[70:73], v[0:15]\nds_read2st64_b64 v[56:59], v76 offset0:41 offset1:42\n"
            "v_exp_f32 v112, v112\nv_exp_f32 v113, v113\nv_exp_f32 v114, v114\nv_exp_f32 v115, v115\nv_cvt_pk_bf16_f32 v48, v112, v113\nv_cvt_pk_bf16_f32 v49, v114, v115\n"
            "v_mfma_f32_32x32x16_bf16 v[16:31], v[90:93], v[70:73], v[16:31]\nds_read2st64_b64 v[90:93], v76 offset0:43 offset1:44\n"
            "v_exp_f32 v116, v116\nv_exp_f32 v117, v117\nv_exp_f32 v118, v118\nv_exp_f32 v119, v119\nv_cvt_pk_bf16_f32 v50, v116, v117\nv_cvt_pk_bf16_f32 v51, v118, v119\n"
            "s_waitcnt lgkmcnt(2)\nv_mfma_f32_32x32x16_bf16 v[32:47], v[66:69], v[52:55], v[32:47]\n"
            "v_exp_f32 v120, v120\nv_exp_f32 v121, v121\nv_exp_f32 v122, v122\nv_exp_f32 v123, v123\nv_cvt_pk_bf16_f32 v52, v120, v121\nv_cvt_pk_bf16_f32 v53, v122, v123\n"
            "v_mfma_f32_32x32x16_bf16 v[32:47], v[62:65], v[48:51], v[32:47]\ns_mov_b64 exec, s[36:37]\nds_read_b128 v[66:69], v78 offset:9120\nds_read_b128 v[62:65], v78 offset:18944\ns_mov_b64 exec, -1\n"
            "v_exp_f32 v124, v124\nv_exp_f32 v125, v125\nv_exp_f32 v126, v126\nv_exp_f32 v127, v127\nv_cvt_pk_bf16_f32 v54, v124, v125\nv_cvt_pk_bf16_f32 v55, v126, v127\n"
            "s_branch 12f\n10:\n"
            "v_mfma_f32_32x32x4_2b_bf16 v[96:127], v[60:61], v[74:75], 0\nds_read_b32 v60, v77 offset:512\n"
            "v_exp_f32 v0, v0\nv_exp_f32 v1, v1\nv_exp_f32 v2, v2\nv_exp_f32 v3, v3\nv_cvt_pk_bf16_f32 v48, v0, v1\nv_cvt_pk_bf16_f32 v49, v2, v3\n"
            "v_exp_f32 v4, v4\nv_exp_f32 v5, v5\nv_exp_f32 v6, v6\nv_exp_f32 v7, v7\nv_cvt_pk_bf16_f32 v50, v4, v5\nv_cvt_pk_bf16_f32 v51, v6, v7\n"
            "s_waitcnt lgkmcnt(1)\nv_mfma_f32_32x32x16_bf16 v[32:47], v[66:69], v[52:55], v[32:47]\n"
            "v_exp_f32 v8, v8\nv_exp_f32 v9, v9\nv_exp_f32 v10, v10\nv_exp_f32 v11, v11\nv_cvt_pk_bf16_f32 v52, v8, v9\nv_cvt_pk_bf16_f32 v53, v10, v11\n"
            "v_mfma_f32_32x32x16_bf16 v[32:47], v[62:65], v[48:51], v[32:47]\ns_mov_b64 exec, s[36:37]\nds_read_b128 v[66:69], v78 offset:608\nds_read_b128 v[62:65], v78 offset:1216\ns_mov_b64 exec, -1\n"
            "v_exp_f32 v12, v12\nv_exp_f32 v13, v13\nv_exp_f32 v14, v14\nv_exp_f32 v15, v15\nv_cvt_pk_bf16_f32 v54, v12, v13\nv_cvt_pk_bf16_f32 v55, v14, v15\n"
            "v_mfma_f32_32x32x16_bf16 v[96:111], v[56:59], v[70:73], v[96:111]\nds_read2st64_b64 v[56:59], v76 offset0:8 offset1:9\n"
            "v_exp_f32 v16, v16\nv_exp_f32 v17, v17\nv_exp_f32 v18, v18\nv_exp_f32 v19, v19\nv_cvt_pk_bf16_f32 v48, v16, v17\nv_cvt_pk_bf16_f32 v49, v18, v19\n"
            "v_mfma_f32_32x32x16_bf16 v[112:127], v[90:93], v[70:73], v[112:127]\nds_read2st64_b64 v[90:93], v76 offset0:10 offset1:11\n"
            "v_exp_f32 v20, v20\nv_exp_f32 v21, v21\nv_exp_f32 v22, v22\nv_exp_f32 v23, v23\nv_cvt_pk_bf16_f32 v50, v20, v21\nv_cvt_pk_bf16_f32 v51, v22, v23\n"
            "s_waitcnt lgkmcnt(2)\nv_mfma_f32_32x32x16_bf16 v[32:47], v[66:69], v[52:55], v[32:47]\n"
            "v_exp_f32 v24, v24\nv_exp_f32 v25, v25\nv_exp_f32 v26, v26\nv_exp_f32 v27, v27\nv_cvt_pk_bf16_f32 v52, v24, v25\nv_cvt_pk_bf16_f32 v53, v26, v27\n"
            "v_mfma_f32_32x32x16_bf16 v[32:47], v[62:65], v[48:51], v[32:47]\ns_mov_b64 exec, s[36:37]\nds_read_b128 v[66:69], v78 offset:1824\nds_read_b128 v[62:65], v78 offset:2432\ns_mov_b64 exec, -1\n"
            "v_exp_f32 v28, v28\nv_exp_f32 v29, v29\nv_exp_f32 v30, v30\nv_exp_f32 v31, v31\nv_cvt_pk_bf16_f32 v54, v28, v29\nv_cvt_pk_bf16_f32 v55, v30, v31\n"
            "v_mfma_f32_32x32x4_2b_bf16 v[0:31], v[60:61], v[74:75], 0\nds_read_b32 v60, v77 offset:768\n"
            "v_exp_f32 v96, v96\nv_exp_f32 v97, v97\nv_exp_f32 v98, v98\nv_exp_f32 v99, v99\nv_cvt_pk_bf16_f32 v48, v96, v97\nv_cvt_pk_bf16_f32 v49, v98, v99\n"
            "v_exp_f32 v100, v100\nv_exp_f32 v101, v101\nv_exp_f32 v102, v102\nv_exp_f32 v103, v103\nv_cvt_pk_bf16_f32 v50, v100, v101\nv_cvt_pk_bf16_f32 v51, v102, v103\n"
            "s_waitcnt lgkmcnt(1)\nv_mfma_f32_32x32x16_bf16 v[32:47], v[66:69], v[52:55], v[32:47]\n"
            "v_exp_f32 v104, v104\nv_exp_f32 v105, v105\nv_exp_f32 v106, v106\nv_exp_f32 v107, v107\nv_cvt_pk_bf16_f32 v52, v104, v105\nv_cvt_pk_bf16_f32 v53, v106, v107\n"
            "v_mfma_f32_32x32x16_bf16 v[32:47], v[62:65], v[48:51], v[32:47]\ns_mov_b64 exec, s[36:37]\nds_read_b128 v[66:69], v78 offset:3040\nds_read_b128 v[62:65], v78 offset:3648\ns_mov_b64 exec, -1\n"
            "v_exp_f32 v108, v108\nv_exp_f32 v109, v109\nv_exp_f32 v110, v110\nv_exp_f32 v111, v111\nv_cvt_pk_bf16_f32 v54, v108, v109\nv_cvt_pk_bf16_f32 v55, v110, v111\n"
            "v_mfma_f32_32x32x16_bf16 v[0:15], v[56:59], v[70:73], v[0:15]\nds_read2st64_b64 v[56:59], v76 offset0:12 offset1:13\n"
            "v_exp_f32 v112, v112\nv_exp_f32 v113, v113\nv_exp_f32 v114, v114\nv_exp_f32 v115, v115\nv_cvt_pk_bf16_f32 v48, v112, v113\nv_cvt_pk_bf16_f32 v49, v114, v115\n"
            "v_mfma_f32_32x32x16_bf16 v[16:31], v[90:93], v[70:73], v[16:31]\nds_read2st64_b64 v[90:93], v76 offset0:14 offset1:15\n"
            "v_exp_f32 v116, v116\nv_exp_f32 v117, v117\nv_exp_f32 v118, v118\nv_exp_f32 v119, v119\nv_cvt_pk_bf16_f32 v50, v116, v117\nv_cvt_pk_bf16_f32 v51, v118, v119\n"
            "s_waitcnt lgkmcnt(2)\nv_mfma_f32_32x32x16_bf16 v[32:47], v[66:69], v[52:55], v[32:47]\n"
            "v_exp_f32 v120, v120\nv_exp_f32 v121, v121\nv_exp_f32 v122, v122\nv_exp_f32 v123, v123\nv_cvt_pk_bf16_f32 v52, v120, v121\nv_cvt_pk_bf16_f32 v53, v122, v123\n"
            "v_mfma_f32_32x32x16_bf16 v[32:47], v[62:65], v[48:51], v[32:47]\ns_mov_b64 exec, s[36:37]\nds_read_b128 v[66:69], v78 offset:4256\nds_read_b128 v[62:65], v78 offset:4864\ns_mov_b64 exec, -1\n"
            "v_exp_f32 v124, v124\nv_exp_f32 v125, v125\nv_exp_f32 v126, v126\nv_exp_f32 v127, v127\nv_cvt_pk_bf16_f32 v54, v124, v125\nv_cvt_pk_bf16_f32 v55, v126, v127\n"
            "v_mfma_f32_32x32x4_2b_bf16 v[96:127], v[60:61], v[74:75], 0\nds_read_b32 v60, v77 offset:18944\n"
            "v_exp_f32 v0, v0\nv_exp_f32 v1, v1\nv_exp_f32 v2, v2\nv_exp_f32 v3, v3\nv_cvt_pk_bf16_f32 v48, v0, v1\nv_cvt_pk_bf16_f32 v49, v2, v3\n"
            "v_exp_f32 v4, v4\nv_exp_f32 v5, v5\nv_exp_f32 v6, v6\nv_exp_f32 v7, v7\nv_cvt_pk_bf16_f32 v50, v4, v5\nv_cvt_pk_bf16_f32 v51, v6, v7\n"
            "s_waitcnt lgkmcnt(1)\nv_mfma_f32_32x32x16_bf16 v[32:47], v[66:69], v[52:55], v[32:47]\n"
            "v_exp_f32 v8, v8\nv_exp_f32 v9, v9\nv_exp_f32 v10, v10\nv_exp_f32 v11, v11\nv_cvt_pk_bf16_f32 v52, v8, v9\nv_cvt_pk_bf16_f32 v53, v10, v11\n"
            "v_mfma_f32_32x32x16_bf16 v[32:47], v[62:65], v[48:51], v[32:47]\ns_mov_b64 exec, s[36:37]\nds_read_b128 v[66:69], v78 offset:5472\nds_read_b128 v[62:65], v78 offset:6080\ns_mov_b64 exec, -1\n"
            "v_exp_f32 v12, v12\nv_exp_f32 v13, v13\nv_exp_f32 v14, v14\nv_exp_f32 v15, v15\nv_cvt_pk_bf16_f32 v54, v12, v13\nv_cvt_pk_bf16_f32 v55, v14, v15\n"
            "11:\n"
            "v_mfma_f32_32x32x16_bf16 v[96:111], v[56:59], v[70:73], v[96:111]\nds_read2st64_b64 v[56:59], v76 offset0:37 offset1:38\n"
            "v_exp_f32 v16, v16\nv_exp_f32 v17, v17\nv_exp_f32 v18, v18\nv_exp_f32 v19, v19\nv_cvt_pk_bf16_f32 v48, v16, v17\nv_cvt_pk_bf16_f32 v49, v18, v19\n"
            "v_mfma_f32_32x32x16_bf16 v[112:127], v[90:93], v[70:73], v[112:127]\nds_read2st64_b64 v[90:93], v76 offset0:39 offset1:40\n"
            "v_exp_f32 v20, v20\nv_exp_f32 v21, v21\nv_exp_f32 v22, v22\nv_exp_f32 v23, v23\nv_cvt_pk_bf16_f32 v50, v20, v21\nv_cvt_pk_bf16_f32 v51, v22, v23\n"
            "s_waitcnt lgkmcnt(2)\nv_mfma_f32_32x32x16_bf16 v[32:47], v[66:69], v[52:55], v[32:47]\n"
            "v_exp_f32 v24, v24\nv_exp_f32 v25, v25\nv_exp_f32 v26, v26\nv_exp_f32 v27, v27\nv_cvt_pk_bf16_f32 v52, v24, v25\nv_cvt_pk_bf16_f32 v53, v26, v27\n"
            "v_mfma_f32_32x32x16_bf16 v[32:47], v[62:65], v[48:51], v[32:47]\ns_mov_b64 exec, s[36:37]\nds_read_b128 v[66:69], v78 offset:6688\nds_read_b128 v[62:65], v78 offset:7296\ns_mov_b64 exec, -1\n"
            "v_exp_f32 v28, v28\nv_exp_f32 v29, v29\nv_exp_f32 v30, v30\nv_exp_f32 v31, v31\nv_cvt_pk_bf16_f32 v54, v28, v29\nv_cvt_pk_bf16_f32 v55, v30, v31\n"
            "v_mfma_f32_32x32x4_2b_bf16 v[0:31], v[60:61], v[74:75], 0\nds_read_b32 v60, v77 offset:19200\n"
            "v_exp_f32 v96, v96\nv_exp_f32 v97, v97\nv_exp_f32 v98, v98\nv_exp_f32 v99, v99\nv_cvt_pk_bf16_f32 v48, v96, v97\nv_cvt_pk_bf16_f32 v49, v98, v99\n"
            "v_exp_f32 v100, v100\nv_exp_f32 v101, v101\nv_exp_f32 v102, v102\nv_exp_f32 v103, v103\nv_cvt_pk_bf16_f32 v50, v100, v101\nv_cvt_pk_bf16_f32 v51, v102, v103\n"
            "s_waitcnt lgkmcnt(1)\nv_mfma_f32_32x32x16_bf16 v[32:47], v[66:69], v[52:55], v[32:47]\n"
            "v_exp_f32 v104, v104\nv_exp_f32 v105, v105\nv_exp_f32 v106, v106\nv_exp_f32 v107, v107\nv_cvt_pk_bf16_f32 v52, v104, v105\nv_cvt_pk_bf16_f32 v53, v106, v107\n"
            "v_mfma_f32_32x32x16_bf16 v[32:47], v[62:65], v[48:51], v[32:47]\ns_mov_b64 exec, s[36:37]\nds_read_b128 v[66:69], v78 offset:7904\nds_read_b128 v[62:65], v78 offset:8512\ns_mov_b64 exec, -1\n"
            "v_exp_f32 v108, v108\nv_exp_f32 v109, v109\nv_exp_f32 v110, v110\nv_exp_f32 v111, v111\nv_cvt_pk_bf16_f32 v54, v108, v109\nv_cvt_pk_bf16_f32 v55, v110, v111\n"
            "v_mfma_f32_32x32x16_bf16 v[0:15], v[56:59], v[70:73], v[0:15]\nds_read2st64_b64 v[56:59], v76 offset0:41 offset1:42\n"
            "v_exp_f32 v112, v112\nv_exp_f32 v113, v113\nv_exp_f32 v114, v114\nv_exp_f32 v115, v115\nv_cvt_pk_bf16_f32 v48, v112, v113\nv_cvt_pk_bf16_f32 v49, v114, v115\n"
            "v_mfma_f32_32x32x16_bf16 v[16:31], v[90:93], v[70:73], v[16:31]\nds_read2st64_b64 v[90:93], v76 offset0:43 offset1:44\n"
            "v_exp_f32 v116, v116\nv_exp_f32 v117, v117\nv_exp_f32 v118, v118\nv_exp_f32 v119, v119\nv_cvt_pk_bf16_f32 v50, v116, v117\nv_cvt_pk_bf16_f32 v51, v118, v119\n"
            "s_waitcnt lgkmcnt(2)\nv_mfma_f32_32x32x16_bf16 v[32:47], v[66:69], v[52:55], v[32:47]\n"
            "v_exp_f32 v120, v120\nv_exp_f32 v121, v121\nv_exp_f32 v122, v122\nv_exp_f32 v123, v123\nv_cvt_pk_bf16_f32 v52, v120, v121\nv_cvt_pk_bf16_f32 v53, v122, v123\n"
            "v_mfma_f32_32x32x16_bf16 v[32:47], v[62:65], v[48:51], v[32:47]\ns_mov_b64 exec, s[36:37]\nds_read_b128 v[66:69], v78 offset:9120\nds_read_b128 v[62:65], v78 offset:18944\ns_mov_b64 exec, -1\n"
            "v_exp_f32 v124, v124\nv_exp_f32 v125, v125\nv_exp_f32 v126, v126\nv_exp_f32 v127, v127\nv_cvt_pk_bf16_f32 v54, v124, v125\nv_cvt_pk_bf16_f32 v55, v126, v127\n"
            "12:\ns_waitcnt lgkmcnt(0)\nv_mfma_f32_32x32x16_bf16 v[32:47], v[66:69], v[52:55], v[32:47]\ns_setprio 0\ns_waitcnt vmcnt(0)\ns_nop 7\ns_nop 7\n"
            : "={v[32:47]}"(O), "+{v76}"(a0), "+{v77}"(a1), "+{v78}"(a2), [early] "=&s"(early)
            : "{v[70:73]}"(qm), "{v74}"(q2), "{v79}"(v16), "{v80}"(pfv), [wid] "s"(wid), [kv] "s"(skv), [pf] "s"(spf), [pfs] "s"(pfs), [pfn] "s"(pfn), [p0] "s"((wid + 24) * 1024), [lds] "s"(lds0),
              [ws] "s"(sw), [opx] "s"(opx), "{v88}"(vl), "{v89}"(vf), [fw] "s"(WS_FW), [tl] "s"(tl), [th] "s"(th), [wp] "s"(wp)
            : "memory", "scc", "vcc", "s36", "s37", "s38", "s39", "s40", "s41", "s42", "s43", "s44", "s45", "v0", "v1", "v2", "v3", "v4", "v5", "v6", "v7", "v8", "v9", "v10", "v11", "v12", "v13", "v14", "v15", "v16", "v17", "v18", "v19", "v20", "v21", "v22", "v23", "v24", "v25", "v26", "v27", "v28", "v29", "v30", "v31", "v48", "v49", "v50", "v51", "v52", "v53", "v54", "v55", "v56", "v57", "v58", "v59", "v60", "v61", "v62", "v63", "v64", "v65", "v66", "v67", "v68", "v69", "v75", "v81", "v82", "v83", "v84", "v85", "v90", "v91", "v92", "v93", "v96", "v97", "v98", "v99", "v100", "v101", "v102", "v103", "v104", "v105", "v106", "v107", "v108", "v109", "v110", "v111", "v112", "v113", "v114", "v115", "v116", "v117", "v118", "v119", "v120", "v121", "v122", "v123", "v124", "v125", "v126", "v127");
    }
    float l = lowhalf(O[10]);
    const bool bad = !(l < 1.8e19f && l > 1e-18f);
    if (__builtin_expect(__any(bad), 0)) {
        const u32x2 qr = {h ? 0u : q2, 0u}; float m = -INFINITY; const unsigned vs = 9216 + h * 304 + (r < 19 ? r : r - 16) * 16;
        ROLL for (int pass = 0; pass < 2; pass++) {
            O = (f32x16){};
            ROLL for (int t = 0; t < 64; t++) {
                const unsigned cb = (t >> 3) * CHUNK; const int i = t & 7;
                const u32x2 k0 = ld64(skv, ka, cb + i * 1024), k1 = ld64(skv, ka + 512, cb + i * 1024), kr = {ld32(skv, kra, cb + i * 128), 0u};
                f32x16 s = MFMA32(__builtin_bit_cast(bf16x8, (u32x4){k0[0], k0[1], k1[0], k1[1]}), qm, (f32x16){});
                s = MFMA32K8(__builtin_bit_cast(s16x4, kr), __builtin_bit_cast(s16x4, qr), s);
                if (pass == 0) { UNR for (int e = 0; e < 16; e++) m = __builtin_fmaxf(m, s[e]); }
                else {
                    u32x4 p0, p1;
                    UNR for (int e = 0; e < 4; e++) { p0[e] = cvtpk(__builtin_amdgcn_exp2f(s[2 * e] - m), __builtin_amdgcn_exp2f(s[2 * e + 1] - m)); p1[e] = cvtpk(__builtin_amdgcn_exp2f(s[8 + 2 * e] - m), __builtin_amdgcn_exp2f(s[9 + 2 * e] - m)); }
                    O = MFMA32(__builtin_bit_cast(bf16x8, ld128(skv, vs, cb + i * 1216)), __builtin_bit_cast(bf16x8, p0), O);
                    O = MFMA32(__builtin_bit_cast(bf16x8, ld128(skv, vs + 608, cb + i * 1216)), __builtin_bit_cast(bf16x8, p1), O);
                }
            }
            m = swapmax32(m);
        }
        l = lowhalf(O[10]);
    }

    const float il = __builtin_amdgcn_rcpf(l);
#define OV(i) min(cvtpk(O[i] * il, O[(i) + 1] * il), 0xfffffffeu)
    st128((u32x4){OV(0), OV(2), OV(4), OV(6)}, sw, vm, om);
    st32(OV(8), sw, vr, orr);
#undef OV
    BAR();
    return early;
}

DI void wo_loader(char* lds, const bf16_t* x, int bid, int lw, int lane) {
    const int idx = bid >> 3, st = (bid & 7) * 4 + idx / 6, nt = idx % 6; const unsigned lds0 = (unsigned)(uintptr_t)lds;
    const srd_t sx = mksrd(x, S * D * 2);
    UNR for (int i = 0; i < 3; i++) { const int c = i * 64 + lane, rl = (c * 171) >> 11; dma(lds0 + O_X + (3 * lw + i) * 1024, (st * 64 + 16 * lw + rl) * 1152 + nt * 192 + (c - 12 * rl) * 16, sx, 0); }
    waitv<0>(); BAR();
    BAR(); BAR();
}
DI void wo_mfma(char* lds, char* ws, srd_t sw, unsigned tl_, unsigned th_, unsigned early, int bid, int wid, int lane) {
    const int idx = bid >> 3, st = (bid & 7) * 4 + idx / 6, nt = idx % 6;
    const int s = lane & 15, g = lane >> 4, q = (lane >> 2) & 3, p = lane & 3, kh = wid & 1, s16 = st * 4 + wid;
    const unsigned be = kh * 55296 + (4 * g + q) * 192 + (g & 1) * 32 + 8 * p, bo = be - (g & 1) * 64;
    unsigned ho[6];
    UNR for (int n = 0; n < 6; n++) { const int t16 = nt * 6 + n, dT = t16 / 9, tl = t16 - 9 * dT;
        ho[n] = dT * 4608 + (tl < 8 ? (tl >> 1) * 1024 + ((2 * (tl & 1) + (g >> 1)) * 16 + s) * 16 + (g & 1) * 8 : 4096 + lane * 8); }
    const unsigned ov[2] = {WS_OF + s16 * 18432 + lane * 16, WS_OF + (s16 ^ 1) * 18432 + lane * 16};
    if (!early) {
        ROLL for (unsigned spins = 0; spins < SPIN_CAP; spins++) { const u32x2 fw = image_flags(sw, lane); if (__all(fw[0] == tl_ && fw[1] == th_)) break; }
        ROLL for (int i = 0; i < 27; i++) dma_dev((unsigned)(uintptr_t)lds + (4 * i + wid) * 1024, lane * 16, sw, WS_WP + nt * O_X + (4 * i + wid) * 1024);
        waitv<0>();
    }
    ROLL for (unsigned spins = 0; spins < SPIN_CAP; spins++) {
        asm volatile("" ::: "memory");
        const u32x4 a = ld128(sw, ov[0], 16384), b = ld128(sw, ov[0], 17408);
        if (__all(max(max(max(a[0], a[1]), max(a[2], a[3])), max(max(b[0], b[1]), max(b[2], b[3]))) != ~0u)) break;
    }
    f32x4 acc[2][6]; u32x2 xv[6];
    LAS const char* pe = (LAS const char*)lds + be; LAS const char* po = (LAS const char*)lds + bo;
    ROLL for (unsigned spins = 0; spins < SPIN_CAP; spins++) {
        asm volatile("" ::: "memory");
        u32x4 of[9][2];
#define OLD(b) do { UNR for (int e = 1; e >= 0; e--) { of[b][e] = ld128(sw, ov[e] + ((b) & 3) * 1024, kh * 9216 + ((b) >> 2) * 4096); SB(); } } while (0)
        UNR for (int b = 0; b < 4; b++) OLD(b);
        if (spins == 0) { BAR(); }
        UNR for (int n = 0; n < 6; n++) acc[0][n] = acc[1][n] = (f32x4){0.f, 0.f, 0.f, 0.f};
        bf16x8 af[2][6]; unsigned mx = 0;
#define ORD(b) do { UNR for (int n = 0; n < 6; n++) af[(b) & 1][n] = tr2(((n & 1) ? po : pe) + (b) * 6144 + n * 32, 3072); } while (0)
        ORD(0);
        UNR for (int b = 0; b < 9; b++) {
            SB();
            if (b + 4 < 9) OLD(b + 4);
            if (b + 1 < 9) ORD(b + 1); else UNR for (int n = 0; n < 6; n++) xv[n] = *(LAS const u32x2*)((LAS const char*)lds + O_X + (wid * 16 + s) * 192 + n * 32 + g * 8);
            UNR for (int e = 1; e >= 0; e--) {
                UNR for (int n = 0; n < 6; n++) acc[e][n] = MFMA16(af[b & 1][n], __builtin_bit_cast(bf16x8, of[b][e]), acc[e][n]);
                mx = max(max(max(mx, of[b][e][0]), of[b][e][1]), max(of[b][e][2], of[b][e][3]));
            }
            UNR for (int n = 0; n < 12; n++) { SGB(0x8, 1); SGB(0x100, 1); }
        }
#undef ORD
#undef OLD
        if (__all(mx != ~0u)) break;
    }
    { LAS f32x4* ex = (LAS f32x4*)(lds + O_E) + wid * 6 * 64 + lane;
      UNR for (int n = 0; n < 6; n++) ex[n * 64] = acc[1][n];
      WAITL(); BAR();
      LAS const f32x4* ey = (LAS const f32x4*)(lds + O_E) + (wid ^ 1) * 6 * 64 + lane;
      UNR for (int n = 0; n < 6; n++) acc[0][n] += ey[n * 64]; }
    f32x2 hv[6][2], a1 = {0.f, 0.f}, a2 = {0.f, 0.f};
    UNR for (int n = 0; n < 6; n++) {
        hv[n][0] = lo2(acc[0][n]) + unpk(xv[n][0]); hv[n][1] = hi2(acc[0][n]) + unpk(xv[n][1]);
        UNR for (int e = 0; e < 2; e++) { a1 += hv[n][e]; a2 = pfma(hv[n][e], hv[n][e], a2); }
    }
    const float s1 = swapadd32(swapadd16(a1[0] + a1[1])), s2 = swapadd32(swapadd16(a2[0] + a2[1]));
    const unsigned gs = WS_ST + s16 * 768 + s * 8;
    { unsigned b2 = __float_as_uint(s2); if (b2 == ~0u) b2 = 0x7fc00000u; st64((u32x2){__float_as_uint(s1), b2}, sw, g == 0 ? gs + nt * 128 : OOB, 0); }
    UNR for (int n = 0; n < 6; n++) st64((u32x2){cvt2(hv[n][0]), cvt2(hv[n][1])}, sw, ho[n], WS_HF + s16 * 18432);
    u32x2 w0, w1;
    ROLL for (unsigned spins = 0; spins < SPIN_CAP; spins++) {
        asm volatile("" ::: "memory");
        w0 = ld64(sw, gs + g * 128, 0); w1 = ld64(sw, g < 2 ? gs + g * 128 + 512 : OOB, 0);
        if (__all(w0[1] != ~0u && w1[1] != ~0u)) break;
    }
    const float t1 = swapadd32(swapadd16(__uint_as_float(w0[0]) + __uint_as_float(w1[0]))), t2 = swapadd32(swapadd16(__uint_as_float(w0[1]) + __uint_as_float(w1[1])));
    const float m = t1 * (1.f / D), var = __builtin_fmaxf(__builtin_fmaf(-m, m, t2 * (1.f / D)), 0.f);
    const float r1 = __builtin_amdgcn_rsqf(var + EPS), rr = r1 * __builtin_amdgcn_rsqf(var * r1 * r1 + EPS);
    LAS char* tb = (LAS char*)lds + O_T + wid * 3328;
    const f32x2 r2 = splat(rr), mr = splat(-m * rr);
    UNR for (int n = 0; n < 6; n++) *(LAS u32x2*)(tb + s * 208 + n * 32 + g * 8) = (u32x2){cvt2(pfma(hv[n][0], r2, mr)), cvt2(pfma(hv[n][1], r2, mr))};
    WAITL();
    UNR for (int dp = 0; dp < 3; dp++)
        st128(__builtin_bit_cast(u32x4, tr2(tb + (4 * (g & 1) + q) * 208 + dp * 64 + (g >> 1) * 32 + p * 8, 8 * 208)), sw, (lane >> 5) * 1024 + (lane & 31) * 16, WS_HH + ((nt * 64 + (s16 >> 1)) * 6 + 2 * dp) * 1024 + (s16 & 1) * 512);
    BAR();
    arrive4(lds, 0, sw, lane < 8 ? lane * 1024 : OOB, WS_F3 + (nt * 64 + (s16 >> 4)) * 128, lane);
}

constexpr int A_NS = 5, A_H = A_NS * 16384;
DI void up_loader(char* lds, char* ws, const bf16_t* w1, srd_t sw, int bid, int lw, int lane) {
    const int L = (bid & 7) * 27 + (bid >> 3), ft = L / 6, dT = L % 6, rq = lane >> 4, c = lane & 15; const unsigned lds0 = (unsigned)(uintptr_t)lds;
    const srd_t s1 = mksrd(w1, S * F * 2), sh = mksrd(ws + WS_HH, S * D * 2);
    unsigned vw[2]; const unsigned vh = lane * 16;
    UNR for (int e = 0; e < 2; e++) vw[e] = (16 * lw + rq) * (F * 2) + ft * 256 + ((((c >> 1) ^ (4 * e + rq)) * 2) + (c & 1)) * 16;
    auto iw = [&](int t, int slot) { UNR for (int i = 0; i < 4; i++) dma(lds0 + slot * 16384 + (16 * lw + 4 * i) * 256, vw[i & 1], s1, (t * 64 + 4 * i) * (F * 2)); };
    auto ia = [&](int t, int slot) { UNR for (int j = 0; j < 3; j++) dma_dev(lds0 + A_H + slot * 12288 + (lw * 3 + j) * 1024, vh, sh, (dT * 32 + t) * 12288 + (lw * 3 + j) * 1024); };
    UNR for (int t = 0; t < A_NS; t++) iw(t, t);
    BAR();
    ring_rest<32, A_NS, 1, 4, 3>(iw, ia);
    BAR(); BAR();
}
DI void up_mfma(char* lds, char* ws, const bf16_t* b1, srd_t sw, int bid, int wid, int lane) {
    const int L = (bid & 7) * 27 + (bid >> 3), ft = L / 6, dT = L % 6;
    const int wf = wid & 1, kh = wid >> 1, g = lane >> 4, q = (lane >> 2) & 3, p = lane & 3, r7 = 4 * (g & 1) + q;
    const int f0 = ft * 128 + wf * 64 + kh * 32;
    u32x2 bb[2];
    UNR for (int i = 0; i < 2; i++) bb[i] = *(const u32x2*)(b1 + f0 + 16 * i + 4 * g);
    unsigned aoff[4], uo[6];
    UNR for (int i = 0; i < 4; i++) aoff[i] = kh * 8192 + (16 * (g >> 1) + r7) * 256 + (((wf * 4 + (i ^ (2 * kh))) ^ r7) * 32) + p * 8;
    const unsigned boff = A_H + kh * 6144 + lane * 16;
    UNR for (int j = 0; j < 6; j++) { const int t16 = dT * 6 + j, dT9 = t16 / 9, tn = t16 - 9 * dT9, d = lane & 15;
        uo[j] = dT9 * (144 * 9216) + g * 256 + (tn < 8 ? ((tn & 6) + ((d >> 2) & 1)) * 1024 + (8 * (tn & 1) + 4 * (d >> 3) + (d & 3)) * 16 : 8192 + d * 16); }
    f32x4 acc[4][6];
    UNR for (int j = 0; j < 6; j++) { UNR for (int i = 0; i < 2; i++) acc[i][j] = (f32x4){blo(bb[i][0]), bhi(bb[i][0]), blo(bb[i][1]), bhi(bb[i][1])}; acc[2][j] = acc[3][j] = (f32x4){0.f, 0.f, 0.f, 0.f}; }
    bf16x8 xa[4], xb[6], ya[4], yb[6];
    UNR for (int i = 0; i < 4; i++) ya[i] = Z8;
    UNR for (int j = 0; j < 6; j++) yb[j] = Z8;
#define ARD(A, B) do { A[3] = tr2(bw + aoff[3], 2048); UNR for (int j = 0; j < 6; j++) B[j] = rd128(bh + j * 1024); UNR for (int i = 2; i >= 0; i--) A[i] = tr2(bw + aoff[i], 2048); } while (0)
#define AMM(A, B) do { UNR for (int i = 3; i >= 0; i--) UNR for (int j = 0; j < 6; j++) acc[i][j] = MFMA16(A[i], B[j], acc[i][j]); } while (0)
#define ASG() do { UNR for (int i = 0; i < 14; i++) { SGB(0x8, 1); SGB(0x100, 1); } SGB(0x8, 24); } while (0)
#define ASTAGE(slot, RA, RB, MA, MB) do { BAR(); LAS const char* bw = (LAS const char*)lds + (slot) * 16384; LAS const char* bh = (LAS const char*)lds + boff + (slot) * 12288; \
        SB(); ARD(RA, RB); AMM(MA, MB); ASG(); SB(); } while (0)
#define APAIR(s0, s1) do { ASTAGE(s0, xa, xb, ya, yb); ASTAGE(s1, ya, yb, xa, xb); } while (0)
    const unsigned f3 = WS_F3 + (dT * 64 + (bid & 7) * 8) * 128;
    if (wid == 0) { if (bid >= 192) await_first(sw, f3); await(sw, lane < 8 ? lane * 128 : OOB, f3, 16); }
    BAR();
    APAIR(0, 1);
    ROLL for (int t = 0; t < 3; t++) { APAIR(2, 3); APAIR(4, 0); APAIR(1, 2); APAIR(3, 4); APAIR(0, 1); }
    AMM(ya, yb);
    LAS f32x4* ex = (LAS f32x4*)(lds + 32768) + wid * 12 * 64 + lane;
    UNR for (int i = 1; i >= 0; i--) UNR for (int j = 0; j < 6; j++) ex[(i * 6 + j) * 64] = acc[2 + i][j];
    WAITL(); BAR();
    LAS const f32x4* ey = (LAS const f32x4*)(lds + 32768) + (wid ^ 2) * 12 * 64 + lane;
    f32x4 y[12];
    UNR for (int j = 0; j < 6; j++) { y[j] = ey[j * 64]; y[6 + j] = ey[(6 + j) * 64]; }
    UNR for (int jj = 0; jj < 6; jj += 2) {
        f32x2 u[8], t[8];
        UNR for (int e = 0; e < 2; e++) UNR for (int i = 0; i < 2; i++) { u[4 * e + 2 * i] = lo2(acc[i][jj + e]) + lo2(y[6 * i + jj + e]); u[4 * e + 2 * i + 1] = hi2(acc[i][jj + e]) + hi2(y[6 * i + jj + e]); }
        SB(); UNR for (int i = 0; i < 8; i++) t[i] = u[i] * u[i];
        SB(); UNR for (int i = 0; i < 8; i++) t[i] = pfma(t[i], splat(-2.f * 0.7978845608028654f * 0.044715f * 1.4426950408889634f), splat(-2.f * 0.7978845608028654f * 1.4426950408889634f));
        SB(); UNR for (int i = 0; i < 8; i++) t[i] = t[i] * u[i];
        SB(); UNR for (int i = 0; i < 8; i++) t[i] = (f32x2){__builtin_amdgcn_exp2f(t[i][0]), __builtin_amdgcn_exp2f(t[i][1])};
        SB(); UNR for (int i = 0; i < 8; i++) t[i] = t[i] + splat(1.f);
        SB(); UNR for (int i = 0; i < 8; i++) t[i] = (f32x2){__builtin_amdgcn_rcpf(t[i][0]), __builtin_amdgcn_rcpf(t[i][1])};
        SB(); UNR for (int i = 0; i < 8; i++) t[i] = t[i] * u[i];
        SB();
        UNR for (int e = 0; e < 2; e++) st128((u32x4){cvt2(t[4 * e]), cvt2(t[4 * e + 1]), cvt2(t[4 * e + 2]), cvt2(t[4 * e + 3])}, sw, uo[jj + e], WS_UF + (f0 >> 5) * 9216);
    }
    BAR();
    arrive4(lds, 1, sw, lane < 2 ? lane * 18 * 128 : OOB, WS_F4 + ((ft / 9) * 36 + dT * 3 + (ft % 9) / 3) * 128, lane);
#undef ASTAGE
#undef APAIR
#undef ARD
#undef AMM
#undef ASG
}

DI void up_idle(const bf16_t* w1, srd_t sw, int bid, int wid, int lane) {
    const int xg = bid & 7, j = ((bid >> 3) - 27) * 4 + wid;
    const srd_t s1 = mksrd(w1, S * F * 2); unsigned sink = 0;
    await_first(sw, WS_F3 + (((bid >> 3) - 27) * 64 + xg * 8 + wid) * 128);
    const unsigned long long t0 = __builtin_amdgcn_s_memrealtime();
    while (__builtin_amdgcn_s_memrealtime() - t0 < 100) __builtin_amdgcn_s_sleep(8);
    ROLL for (int k = 0; k < 12; k++) { const int u = k * 20 + j; touch(sink, ((32 - 12 * 2 + u / 10) * 64 + lane) * (F * 2) + ((9 * xg) >> 1) * 256 + (u % 10) * 128, s1, 0); }
    asm volatile("s_waitcnt vmcnt(0)" : "+v"(sink)::"memory");
}

constexpr int B_NS = 4, B_W = B_NS * 18432;
DI void down_loader(char* lds, char* ws, const bf16_t* w2, srd_t sw, int bid, int lw, int lane) {
    const int xg = bid & 7, idx = bid >> 3, kq = xg >> 1, st = (xg & 1) * 8 + (idx >> 2), dT = idx & 3, rq = lane >> 4, c = lane & 15; const unsigned lds0 = (unsigned)(uintptr_t)lds;
    const srd_t s2 = mksrd(w2, S * F * 2), su = mksrd(ws + WS_UF, D * F * 2);
    unsigned vw[2]; const unsigned vu = lane * 16;
    UNR for (int e = 0; e < 2; e++) vw[e] = (kq * 1152 + 16 * lw + rq) * (S * 2) + st * 256 + ((((c >> 1) ^ (4 * e + rq)) * 2) + (c & 1)) * 16;
    auto iw = [&](int t, int slot) { UNR for (int i = 0; i < 4; i++) dma(lds0 + B_W + slot * 16384 + (16 * lw + 4 * i) * 256, vw[i & 1], s2, (t * 64 + 4 * i) * (S * 2)); };
    UNR for (int t = 0; t < B_NS; t++) iw(t, t);
    BAR();
    auto run = [&](auto NU) {
        constexpr int nu = NU.value; const int u0 = nu == 5 ? lw * 5 : 2 + lw * 4;
        ring_rest<18, B_NS, 0, 4, nu>(iw, [&](int t, int slot) { UNR for (int j = 0; j < nu; j++) dma_dev(lds0 + slot * 18432 + (u0 + j) * 1024, vu, su, (dT * 144 + kq * 36 + 2 * t) * 9216 + (u0 + j) * 1024); }); };
    if (lw < 2) run(IC<5>{}); else run(IC<4>{});
}
DI void down_mfma(char* lds, char* ws, const bf16_t* b2, bf16_t* out, srd_t sw, int bid, int wid, int lane) {
    const int xg = bid & 7, idx = bid >> 3, kq = xg >> 1, st = (xg & 1) * 8 + (idx >> 2), dT = idx & 3, tile = st * 4 + dT;
    const int s = lane & 15, g = lane >> 4, q = (lane >> 2) & 3, p = lane & 3, r7 = 4 * (g & 1) + q;
    unsigned boff[2];
    UNR for (int j = 0; j < 2; j++) boff[j] = B_W + (4 * g + q) * 256 + (((wid * 2 + (j ^ (kq >> 1))) ^ r7) * 32) + p * 8;
    const unsigned aoff = lane * 16; const int asw = (kq & 1) * 4096;
    f32x4 acc[9][2];
    UNR for (int i = 0; i < 9; i++) UNR for (int j = 0; j < 2; j++) acc[i][j] = (f32x4){0.f, 0.f, 0.f, 0.f};
    bf16x8 xa[9], xb[2], ya[9], yb[2] = {Z8, Z8};
    UNR for (int i = 0; i < 9; i++) ya[i] = Z8;
#define BRD(A, B, ks) do { B[0] = tr2(bw + boff[0] + (ks) * 8192, 4096); A[0] = rd128(ua + (ks) * 9216); B[1] = tr2(bw + boff[1] + (ks) * 8192, 4096); \
        UNR for (int i = 1; i < 9; i++) A[i] = rd128((i < 4 ? ua : i < 8 ? ub : uc) + (ks) * 9216 + i * 1024); } while (0)
#define BMM(A, B) do { UNR for (int i = 0; i < 9; i++) UNR for (int j = 0; j < 2; j++) acc[i][j] = MFMA16(A[i], B[j], acc[i][j]); } while (0)
#define BSG() do { UNR for (int i = 0; i < 13; i++) { SGB(0x8, 1); SGB(0x100, 1); } SGB(0x8, 18); } while (0)
    const unsigned f4 = WS_F4 + (kq * 36 + (xg & 1) * 18 + ((3 * dT) >> 1) * 3) * 128;
    if (wid == 0) { if (bid >= 216) await_first(sw, f4); await(sw, lane < 2 ? lane * 384 : OOB, f4, 12); }
    BAR();
    const bool odd = kq & 1; const int s16 = st * 8 + wid * 2 + (kq >> 1); const unsigned hf = WS_HF + s16 * 18432 + dT * 4608;
    const u32x4 hp0 = ld128(sw, lane * 16, hf + (kq & 1) * 2048), hp1 = ld128(sw, lane * 16 + 1024, hf + (kq & 1) * 2048);
    const u32x2 hs = ld64(sw, lane * 8, hf + 4096); const float bs = blo(b2[s16 * 16 + s]);
#define BSTAGE(slot) do { BAR(); LAS const char* bw = (LAS const char*)lds + (slot) * 16384; LAS const char* uc = (LAS const char*)lds + aoff + (slot) * 18432; LAS const char* ua = uc + asw; LAS const char* ub = uc - asw; \
        SB(); BRD(xa, xb, 0); BMM(ya, yb); BSG(); SB(); BRD(ya, yb, 1); BMM(xa, xb); BSG(); WAITL(); } while (0)
    BSTAGE(0);
    unsigned rest = 12; if (wid == 0) rest = ld32(sw, lane < 6 ? lane * 128 : 0u, f4);
    BSTAGE(1);
    if (wid == 0 && !__all(rest >= 12)) await(sw, lane < 6 ? lane * 128 : OOB, f4, 12);
    ROLL for (int t = 0; t < 4; t++) { BSTAGE(2); BSTAGE(3); BSTAGE(0); BSTAGE(1); }
#undef BSTAGE
    const unsigned box = (tile * 4 + wid) * 12, v16 = lane * 16, v8e = odd ? OOB : 2048 + lane * 8, v8o = odd ? 2048 + lane * 8 : OOB;
#define CV(v) min(cvt2(v), 0xfffffffeu)
#define PK2(i, j) (u32x4){CV(lo2(acc[i][j])), CV(hi2(acc[i][j])), CV(lo2(acc[(i) + 1][j])), CV(hi2(acc[(i) + 1][j]))}
#define PK1(j) (u32x2){CV(lo2(acc[8][j])), CV(hi2(acc[8][j]))}
#define FIN(i, j) acc[i][j] = MFMA16(ya[i], yb[j], acc[i][j])
    const unsigned o1 = WS_PX + (box + (kq ^ 1) * 3) * 2560, o2 = WS_PX + (box + (kq ^ 2) * 3 + 1) * 2560, o3 = WS_PX + (box + (kq ^ 3) * 3 + 2) * 2560;
    FIN(4, 0); FIN(5, 0); FIN(6, 0); FIN(7, 0); FIN(0, 1); FIN(1, 1); SB();
    FIN(2, 1); FIN(3, 1); FIN(4, 1); FIN(5, 1); st128(PK2(4, 0), sw, v16, o1); SB();
    FIN(6, 1); FIN(7, 1); FIN(8, 0); FIN(8, 1); st128(PK2(6, 0), sw, v16 + 1024, o1); SB();
    FIN(0, 0); FIN(1, 0); FIN(2, 0); FIN(3, 0); st128(PK2(0, 1), sw, v16, o2); SB();
    st64(PK1(0), sw, v8e, o1); st64(PK1(1), sw, 2048 + lane * 8, odd ? o2 : o3);
    st128(PK2(2, 1), sw, v16 + 1024, o2); st128(PK2(4, 1), sw, v16, o3); st128(PK2(6, 1), sw, v16 + 1024, o3);
#undef FIN
    f32x2 r[5][2];
    UNR for (int i = 0; i < 4; i++) { const u32x4 hh = i < 2 ? hp0 : hp1; r[i][0] = lo2(acc[i][0]) + splat(bs) + unpk(hh[2 * (i & 1)]); r[i][1] = hi2(acc[i][0]) + splat(bs) + unpk(hh[2 * (i & 1) + 1]); }
    r[4][0] = lo2(acc[8][0]) + splat(bs) + unpk(hs[0]); r[4][1] = hi2(acc[8][0]) + splat(bs) + unpk(hs[1]);
    u32x4 pa[3][2]; u32x2 pb[3];
    ROLL for (unsigned spins = 0; spins < SPIN_CAP; spins++) {
        asm volatile("" ::: "memory");
        if (__all(ld32(sw, lane < 3 ? lane * 2560 + 2044 : OOB, WS_PX + (box + kq * 3) * 2560) != ~0u)) break;
        __builtin_amdgcn_s_sleep(1);
    }
    ROLL for (unsigned spins = 0; spins < SPIN_CAP; spins++) {
        asm volatile("" ::: "memory");
        UNR for (int c = 0; c < 3; c++) { const unsigned o = WS_PX + (box + kq * 3 + c) * 2560; pa[c][0] = ld128(sw, v16, o); pa[c][1] = ld128(sw, v16 + 1024, o); pb[c] = ld64(sw, v8o, o); }
        SB();
        unsigned mx = 0;
        UNR for (int c = 0; c < 3; c++) { UNR for (int i = 0; i < 2; i++) mx = max(max(max(mx, pa[c][i][0]), pa[c][i][1]), max(pa[c][i][2], pa[c][i][3])); mx = max(max(mx, pb[c][0]), pb[c][1]); }
        if (__all(mx != ~0u)) break;
    }
    UNR for (int c = 0; c < 3; c++) {
        UNR for (int i = 0; i < 4; i++) { r[i][0] += unpk(pa[c][i >> 1][2 * (i & 1)]); r[i][1] += unpk(pa[c][i >> 1][2 * (i & 1) + 1]); }
        r[4][0] += unpk(pb[c][0]); r[4][1] += unpk(pb[c][1]);
    }
    const srd_t so = mksrd(out, S * D * 2); const unsigned ob = (s16 * 16 + s) * 1152 + dT * 288;
    __builtin_amdgcn_raw_buffer_store_b128((u32x4){cvt2(r[0][0]), cvt2(r[0][1]), cvt2(r[1][0]), cvt2(r[1][1])}, so, ob + (kq & 1) * 128 + 16 * g, 0, 0);
    __builtin_amdgcn_raw_buffer_store_b128((u32x4){cvt2(r[2][0]), cvt2(r[2][1]), cvt2(r[3][0]), cvt2(r[3][1])}, so, ob + (kq & 1) * 128 + 64 + 16 * g, 0, 0);
    __builtin_amdgcn_raw_buffer_store_b64((u32x2){cvt2(r[4][0]), cvt2(r[4][1])}, so, odd ? ob + 256 + 8 * g : OOB, 0, 0);
#undef CV
#undef PK2
#undef PK1
#undef BRD
#undef BMM
#undef BSG
}

__global__ __launch_bounds__(512, 2) void k_block(const bf16_t* __restrict__ x, const bf16_t* __restrict__ wqkv, const bf16_t* __restrict__ wo, const bf16_t* __restrict__ w1, const bf16_t* __restrict__ b1,
                                                  const bf16_t* __restrict__ w2, const bf16_t* __restrict__ b2, bf16_t* __restrict__ out, char* __restrict__ ws) {
    __shared__ __attribute__((aligned(16))) char lds[LDS_BYTES];
    const int tid = threadIdx.x, lane = tid & 63, wid = rfl(tid >> 6), bid = blockIdx.x;
    const srd_t sw = mksrd(ws, WS_BYTES);
    const u32x2 ep = ld64(sw, 0, WS_EP);
    if (bid >= 224) phase_spare(lds, x, wqkv, sw, ep, bid - 224, tid, wid, lane); else phase_qkv(lds, x, wqkv, sw, ep, bid, tid, wid, lane);
    const unsigned long long tag = (((unsigned long long)rfl(ep[1]) << 32) | rfl(ep[0])) + 1ull; const unsigned tl = (unsigned)tag, th = (unsigned)(tag >> 32);
    if (tid == 0) *(LAS u32x2*)(lds + LDS_BYTES - 16) = (u32x2){0u, 0u};
    WAITL();
    waitv<0>(); BAR();
    if (wid == 0) st64((u32x2){tl, th}, sw, lane < 8 ? lane * 32768 : OOB, WS_F1 + bid * 128);
    if (bid >= 224) spare_image(wo, sw, tl, th, bid - 224, tid, wid, lane);
    const unsigned early = phase_attn(lds, ws, w1, w2, sw, tl, th, bid, wid, lane);
    if (wid < 4) {
        if (bid < 192) wo_mfma(lds, ws, sw, tl, th, early, bid, wid, lane);
        if (bid < 216) up_mfma(lds, ws, b1, sw, bid, wid, lane);
        else up_idle(w1, sw, bid, wid, lane);
        down_mfma(lds, ws, b2, out, sw, bid, wid, lane);
        if (bid == 0 && wid == 0) st64((u32x2){tl, th}, sw, lane == 0 ? 0u : OOB, WS_EP);
    } else {
        if (bid < 192) wo_loader(lds, x, bid, wid - 4, lane);
        if (bid < 216) up_loader(lds, ws, w1, sw, bid, wid - 4, lane);
        down_loader(lds, ws, w2, sw, bid, wid - 4, lane);
    }
}

extern "C" void kernel_launch(void* const* d_in, const int* in_sizes, int n_in, void* d_out, int out_size, void* d_ws, size_t ws_size, hipStream_t stream) {
    k_block<<<256, 512, 0, stream>>>((const bf16_t*)d_in[0], (const bf16_t*)d_in[1], (const bf16_t*)d_in[2], (const bf16_t*)d_in[4], (const bf16_t*)d_in[5], (const bf16_t*)d_in[6], (const bf16_t*)d_in[7],
                                     (bf16_t*)d_out, (char*)d_ws);
}
```
